```python
import torch

def _softmax(x, axis=-1):
    return torch.softmax(x, dim=axis)
def _gelu(x):
    return torch.nn.functional.gelu(x, approximate="tanh")
def _silu(x):
    return torch.nn.functional.silu(x)
def _sigmoid(x):
    return torch.sigmoid(x)
def _layernorm(x, axis=-1, eps=1e-5):
    m = x.mean(dim=axis, keepdim=True)
    v = x.var(dim=axis, keepdim=True, unbiased=False)
    return (x - m) / torch.sqrt(v + eps)
def _scatter_add(tgt, idx, src, axis=0):
    return tgt.clone().index_add_(axis, idx, src)
def _segment_sum(src, seg, n):
    out = torch.zeros((n,) + src.shape[1:], dtype=src.dtype)
    return out.index_add_(0, seg, src)
def _pad(x, pw):
    flat = tuple(v for pair in reversed(pw) for v in pair)
    return torch.nn.functional.pad(x, flat)

kernel_name = 'interleave_layers__linear_attn_block__linear_kernel_hedgehog__prompt'

def setup_inputs():

    _w = (lambda a: (lambda t: t.to(torch.bfloat16) if t.is_floating_point() else t)(torch.as_tensor(a)))
    def _A_setup():
        B, S, D = 1, 2048, 640
        return {
            "x": _w((lambda *s: torch.randn(*s, dtype=torch.bfloat16) * 0.4)(B, S, D)),
            "wqkv": _w((lambda *s: torch.randn(*s, dtype=torch.bfloat16) * 0.8)(D, 3 * D)),
            "wo": _w((lambda *s: torch.randn(*s, dtype=torch.bfloat16) * 0.8)(D, D)),
        }
    _A_in = _A_setup()

    def _B_setup():
        B, S, D, H = 1, 2048, 640, 32
        Dh = D // H
        return {
            "x": _w((lambda *s: torch.randn(*s, dtype=torch.bfloat16) * 0.4)(B, S, D)),
            "wqkv": _w((lambda *s: torch.randn(*s, dtype=torch.bfloat16) * 0.8)(D, 3 * D)),
            "w_phi": _w((lambda *s: torch.randn(*s, dtype=torch.bfloat16) * 0.8)(H, Dh, Dh) * (Dh ** -0.5)),
            "wo": _w((lambda *s: torch.randn(*s, dtype=torch.bfloat16) * 0.4)(D, D)),
        }
    _B_in = _B_setup()

    return {
        "A_x": _A_in["x"],
        "A_wqkv": _A_in["wqkv"],
        "A_wo": _A_in["wo"],
        "B_x": _B_in["x"],
        "B_wqkv": _B_in["wqkv"],
        "B_w_phi": _B_in["w_phi"],
        "B_wo": _B_in["wo"],
    }

def reference(A_x, A_wqkv, A_wo, B_x, B_wqkv, B_w_phi, B_wo):
    def _A_ref(x, wqkv, wo):
        B, S, D = x.shape
        H = 32; Dh = D // H
        qkv = (lambda x, *d: x.permute(*d))((x @ wqkv).reshape(B, S, 3, H, Dh), 2, 0, 3, 1, 4)
        q, k, v = qkv[0], qkv[1], qkv[2]
        phi = lambda t: torch.where(t > 0, t + 1, torch.exp(t)) + 1e-6
        pq, pk = phi(q), phi(k)
        kv = torch.einsum("bhsd,bhse->bhde", pk, v)
        z = torch.einsum("bhsd->bhd", pk)
        num = torch.einsum("bhqd,bhde->bhqe", pq, kv)
        den = torch.einsum("bhqd,bhd->bhq", pq, z)[..., None] + 1e-6
        o = num / den
        return (lambda x, *d: x.permute(*d))(o, 0, 2, 1, 3).reshape(B, S, D) @ wo

    def _B_ref(x, wqkv, w_phi, wo):
        B, S, D = x.shape
        H = 32; Dh = D // H
        qkv = (lambda x, *d: x.permute(*d))((x @ wqkv).reshape(B, S, 3, H, Dh), 2, 0, 3, 1, 4)
        q, k, v = qkv[0], qkv[1], qkv[2]
        pq = torch.einsum("bhsd,hdf->bhsf", q, w_phi)
        pk = torch.einsum("bhsd,hdf->bhsf", k, w_phi)
        phi_q = torch.concatenate([_softmax(pq, axis=-1), _softmax(-pq, axis=-1)], axis=-1)
        phi_k = torch.concatenate([_softmax(pk, axis=-1), _softmax(-pk, axis=-1)], axis=-1)
        kv = torch.einsum("bhsf,bhsd->bhfd", phi_k, v)
        z  = torch.einsum("bhsf->bhf", phi_k)
        num = torch.einsum("bhqf,bhfd->bhqd", phi_q, kv)
        den = torch.einsum("bhqf,bhf->bhq", phi_q, z)[..., None] + 1e-6
        o = num / den
        return (lambda x, *d: x.permute(*d))(o, 0, 2, 1, 3).reshape(B, S, D) @ wo

    x = A_x
    x = x + _A_ref(_layernorm(x, axis=-1), A_wqkv, A_wo)
    x = x + _B_ref(_layernorm(x, axis=-1), B_wqkv, B_w_phi, B_wo)
    x = x + _A_ref(_layernorm(x, axis=-1), A_wqkv, A_wo)
    x = x + _B_ref(_layernorm(x, axis=-1), B_wqkv, B_w_phi, B_wo)
    return x
```

```cpp
#include <hip/hip_runtime.h>
#include <hip/hip_bf16.h>
#include <stdint.h>

typedef unsigned v4u __attribute__((ext_vector_type(4)));
typedef unsigned v2u __attribute__((ext_vector_type(2)));
typedef short bf16x8 __attribute__((ext_vector_type(8)));
typedef short s16x4 __attribute__((ext_vector_type(4)));
typedef float f32x2 __attribute__((ext_vector_type(2)));
typedef float f32x4 __attribute__((ext_vector_type(4)));
typedef float f32x16 __attribute__((ext_vector_type(16)));
typedef __bf16 bf16x2v __attribute__((ext_vector_type(2)));
#define LAS __attribute__((address_space(3)))
#define GAS __attribute__((address_space(1)))
#define DI __device__ __forceinline__
#define AI __attribute__((always_inline))
typedef __amdgpu_buffer_rsrc_t rsrc_t;

constexpr int SC1 = 16;
constexpr unsigned POISON = 0xFFFFFFFFu;
constexpr unsigned SPIN_CAP = 1u << 16;
constexpr int OOR = 0x7FFF0000;

constexpr int XB_SZ = 2048 * 640 * 2;
constexpr int KVSLOT = 8192;
constexpr int R_X = 0, R_O = XB_SZ, R_KV = 2 * XB_SZ;
constexpr int LSZ = 2 * XB_SZ + 256 * KVSLOT;
constexpr int WT_SZ = 153600, WO_SZ = 102400;
constexpr int OFF_WT = 4 * LSZ;
constexpr int OFF_CS = OFF_WT + 2 * 16 * WT_SZ;
constexpr int OFF_WO = OFF_CS + 256 * 1024;
constexpr int OFF_CTRL = OFF_WO + 2 * 8 * WO_SZ;
constexpr int OFF_DBG = OFF_CTRL + 16384;
constexpr int WS_USED = OFF_DBG + (1 << 20);

constexpr int L_T0 = 0, L_T1 = 40960, L_T2 = 81920, L_T3 = 112640, L_MISC = 153600;
constexpr int L_KV = L_MISC;
constexpr int L_ZERO = L_KV + 4032;
constexpr int L_DUMP = L_ZERO + 96;
constexpr int L_CS = L_DUMP + 16;
constexpr int LDS_BYTES = 163840;

#define OPQ(x) do { x = __builtin_amdgcn_readfirstlane(x); asm volatile("" : "+s"(x)); } while (0)
#define OPQV(x) asm volatile("" : "+v"(x))
#define PINA(x) asm volatile("" : "+a"(x))
#define SB() __builtin_amdgcn_sched_barrier(0)
#define DRAIN() asm volatile("s_waitcnt vmcnt(0)" ::: "memory")
#define VMWAIT(n) asm volatile("s_waitcnt vmcnt(%0)" :: "i"(n) : "memory")
#define NOUNROLL _Pragma("nounroll")
DI v4u ld128(rsrc_t rs, int voff, int soff) { return __builtin_amdgcn_raw_buffer_load_b128(rs, voff, soff, SC1); }
DI v2u ld64(rsrc_t rs, int voff, int soff) { return __builtin_amdgcn_raw_buffer_load_b64(rs, voff, soff, SC1); }
DI v4u ldi(rsrc_t rs, int& voff, int soff, int coff) { OPQV(voff); return ld128(rs, voff + (coff & 4095), soff + (coff & ~4095)); }
DI unsigned np(unsigned u) { return u < 0xFFFFFFFEu ? u : 0xFFFFFFFEu; }
DI void st128(rsrc_t rs, int voff, int soff, v4u v) { v4u q = {v.x, np(v.y), v.z, np(v.w)}; __builtin_amdgcn_raw_buffer_store_b128(q, rs, voff, soff, SC1); }
DI void st64(rsrc_t rs, int voff, int soff, v2u v) { v2u q = {v.x, np(v.y)}; __builtin_amdgcn_raw_buffer_store_b64(q, rs, voff, soff, SC1); }
DI void st128r(rsrc_t rs, int voff, int soff, v4u v) { __builtin_amdgcn_raw_buffer_store_b128(v, rs, voff, soff, SC1); }
DI void poison128(rsrc_t rs, int voff, int soff) { st128r(rs, voff, soff, (v4u){POISON, POISON, POISON, POISON}); }
DI unsigned pk2(float a, float b) { f32x2 v = {a, b}; return __builtin_bit_cast(unsigned, __builtin_convertvector(v, bf16x2v)); }
DI float u2f(unsigned u) { return __builtin_bit_cast(float, u); }
DI unsigned f2u(float f) { return __builtin_bit_cast(unsigned, f); }
DI float rbf(float a) { return u2f(pk2(a, a) & 0xFFFF0000u); }
DI float blo(unsigned u) { return u2f(u << 16); }
DI float bhi(unsigned u) { return u2f(u & 0xFFFF0000u); }
DI float sum2(unsigned u, float acc) { return __builtin_amdgcn_fdot2_f32_bf16(__builtin_bit_cast(bf16x2v, u), __builtin_bit_cast(bf16x2v, 0x3F803F80u), acc, false); }
DI unsigned umax3(unsigned a, unsigned b, unsigned c) { return max(max(a, b), c); }
DI unsigned mx4(unsigned m, v4u v) { return umax3(m, v.y, v.w); }
DI bf16x8 tr2(LAS char* p, int off1, int off2) {
  s16x4 a = __builtin_amdgcn_ds_read_tr16_b64_v4i16((LAS s16x4*)(p + off1));
  s16x4 b = __builtin_amdgcn_ds_read_tr16_b64_v4i16((LAS s16x4*)(p + off2));
  return __builtin_shufflevector(a, b, 0, 1, 2, 3, 4, 5, 6, 7);
}
DI bf16x8 rd(LAS char* p, int off) { return *(LAS bf16x8*)(p + off); }
DI f32x16 mfma32(bf16x8 a, bf16x8 b, f32x16 c) { return __builtin_amdgcn_mfma_f32_32x32x16_bf16(a, b, c, 0, 0, 0); }
DI f32x4 mfma16(bf16x8 a, bf16x8 b, f32x4 c) { return __builtin_amdgcn_mfma_f32_16x16x32_bf16(a, b, c, 0, 0, 0); }
DI bf16x8 asb(v4u v) { return __builtin_bit_cast(bf16x8, v); }
DI float xsum32(float v) { unsigned u = f2u(v); auto r = __builtin_amdgcn_permlane32_swap(u, u, false, false); return u2f(r[0]) + u2f(r[1]); }
DI float xsum16(float v) { unsigned u = f2u(v); auto r = __builtin_amdgcn_permlane16_swap(u, u, false, false); return u2f(r[0]) + u2f(r[1]); }
DI void bar() { asm volatile("s_waitcnt lgkmcnt(0)" ::: "memory"); __builtin_amdgcn_s_barrier(); asm volatile("" ::: "memory"); }
#define MF32(acc, a, b) do { acc = mfma32(a, b, acc); PINA(acc); SB(); } while (0)
DI void stw(LAS char* p, v4u& v) { PINA(v); *(LAS v4u*)p = v; }
template <int I> struct IC { static constexpr int v = I; };
template <int B, int E, class F> DI void sfor(F&& f) { if constexpr (B < E) { f(IC<B>{}); sfor<B + 1, E>(f); } }

#define STAMP(k) do {} while (0)
#define CL 2
#define CSTAMP(k) do {} while (0)
#define DUMP()
#define ZST(i)
#define RCOUNT(k)
#define LSTAMP(k) do { STAMP(1 + lyr * 24 + (k)); if ((k) >= 2 && (k) < 8 && lyr == CL) CSTAMP(112 + (k)); } while (0)

struct Params {
  const unsigned short* x; const unsigned short* wqkv[2]; const unsigned short* wo[2]; const unsigned short* wphi;
  unsigned char* ws; unsigned short* out;
};
struct Ctx { rsrc_t rs; LAS char* L; int pi, pj, th, cs, w, tid; int xo, oo, ko; };

template <int N> DI unsigned chk128(const v4u* x) { unsigned m = 0;
#pragma unroll
  for (int k = 0; k < N; ++k) m = mx4(m, x[k]);
  return m; }
template <int N>
DI void retry128(const Ctx& c, v4u* x, unsigned m, int voff, int soff, int coff, int stride, int kind) {
  if (__builtin_expect(__any(m == POISON), 0)) {
    NOUNROLL for (unsigned spins = 0; spins < SPIN_CAP; ++spins) {
      OPQ(soff);
      RCOUNT(kind);
#pragma unroll
      for (int k = 0; k < N; ++k) x[k] = ld128(c.rs, voff, soff + coff + k * stride);
      if (!__any(chk128<N>(x) == POISON)) break;
      __builtin_amdgcn_s_sleep(2);
    }
  }
}

DI float accv(const f32x16& m, const f32x16& t, int tb, int i) { return i < 16 ? m[i & 15] : t[tb + (i & 3)]; }
template <bool ISB> struct Phi;
template <> struct Phi<false> {
  static constexpr int NGAP = 25;
  unsigned o[10];
  float v[20], y[20], rl;
  template <int Z> DI void gap(const f32x16& m, const f32x16& t, int tb, float rs) {
    if constexpr (Z == 0) { rl = rs * 1.44269504088896f; OPQV(rl); }
    if constexpr (Z < 20) { v[Z] = accv(m, t, tb, Z); OPQV(v[Z]); }
    if constexpr (Z >= 1 && Z < 21) { y[Z - 1] = v[Z - 1] * rl; OPQV(y[Z - 1]); }
    if constexpr (Z >= 2 && Z < 22) { v[Z - 2] = __builtin_amdgcn_fmed3f(__builtin_amdgcn_exp2f(y[Z - 2]), 0.f, 1.f); OPQV(v[Z - 2]); y[Z - 2] = __builtin_amdgcn_fmed3f(y[Z - 2], 1.44269504e-6f, __builtin_inff()); OPQV(y[Z - 2]); }
    if constexpr (Z >= 3 && Z < 23) { v[Z - 3] = fmaf(y[Z - 3], 0.693147180559945f, v[Z - 3]); OPQV(v[Z - 3]); }
    if constexpr (Z >= 5 && Z < 25 && (Z & 1)) { o[(Z - 5) / 2] = pk2(v[Z - 5], v[Z - 4]); OPQV(o[(Z - 5) / 2]); }
  }
  template <int Z> DI void rest(const f32x16& m, const f32x16& t, int tb, float rs) { if constexpr (Z < NGAP) { gap<Z>(m, t, tb, rs); rest<Z + 1>(m, t, tb, rs); } }
};
template <> struct Phi<true> {
  unsigned o[20];
  f32x2 v[10], en[10], sp, sn; float mx, mn, rl;
  static constexpr int E0 = 12;
  template <int Z> DI void gap(const f32x16& m, const f32x16& t, int tb, float rs) {
    if constexpr (Z == 0) { rl = rs * 1.44269504088896f; OPQV(rl); sp = (f32x2){0.f, 0.f}; sn = (f32x2){0.f, 0.f}; }
    if constexpr (Z < 10) { v[Z] = (f32x2){accv(m, t, tb, 2 * Z), accv(m, t, tb, 2 * Z + 1)}; OPQV(v[Z]); }
    if constexpr (Z == 1) { mx = __builtin_amdgcn_fmed3f(v[0][0], v[0][1], __builtin_inff()); mn = __builtin_amdgcn_fmed3f(v[0][0], v[0][1], -__builtin_inff()); OPQV(mx); OPQV(mn); }
    if constexpr (Z >= 2 && Z < 11) { mx = fmaxf(fmaxf(mx, v[Z - 1][0]), v[Z - 1][1]); OPQV(mx); mn = fminf(fminf(mn, v[Z - 1][0]), v[Z - 1][1]); OPQV(mn); }
    if constexpr (Z == 10) { mx = -mx * rl; mn = mn * rl; OPQV(mx); OPQV(mn); }
    if constexpr (Z >= E0 - 1 && ((Z - E0 + 1) & 1) == 0 && (Z - E0 + 1) / 2 < 10) { constexpr int k = (Z - E0 + 1) / 2; arg(k); OPQV(en[k]); OPQV(v[k]); }
    if constexpr (Z >= E0 && (Z - E0) / 2 < 10) {
      constexpr int k = (Z - E0) / 2;
      if constexpr (((Z - E0) & 1) == 0) { v[k] = ex(v[k]); OPQV(v[k]); } else { en[k] = ex(en[k]); OPQV(en[k]); }
    }
  }
  DI void arg(int k) { const f32x2 r2 = {rl, rl}; en[k] = __builtin_elementwise_fma(v[k], -r2, (f32x2){mn, mn}); v[k] = __builtin_elementwise_fma(v[k], r2, (f32x2){mx, mx}); }
  DI f32x2 ex(f32x2 a) { return (f32x2){__builtin_amdgcn_exp2f(a[0]), __builtin_amdgcn_exp2f(a[1])}; }
  template <int Z> DI void rest(const f32x16& m, const f32x16& t, int tb, float rs) {
#pragma unroll
    for (int k = 0; k < 10; ++k) {
      if (E0 - 1 + 2 * k >= Z) arg(k);
      if (E0 + 2 * k >= Z) v[k] = ex(v[k]);
      if (E0 + 2 * k + 1 >= Z) en[k] = ex(en[k]);
    }
#pragma unroll
    for (int k = 0; k < 10; ++k) { sp = k ? sp + v[k] : v[k]; sn = k ? sn + en[k] : en[k]; }
    const float ip = __builtin_amdgcn_rcpf(sp[0] + sp[1]), in = __builtin_amdgcn_rcpf(sn[0] + sn[1]);
#pragma unroll
    for (int k = 0; k < 10; ++k) { const f32x2 a2 = v[k] * ip, b2 = en[k] * in; o[k] = pk2(a2[0], a2[1]); o[10 + k] = pk2(b2[0], b2[1]); }
  }
};

DI void poison_piece(const Ctx& c, int lb, int n, int& l16, int mixv) {
  OPQV(l16);
  if (n < 2) poison128(c.rs, l16 + n * 1024, lb + c.xo + (c.w & 1) * 2304);
  else if (n < 4) poison128(c.rs, l16, lb + c.oo + (n - 2) * 20480);
  else if (n == 4) poison128(c.rs, mixv, lb);
  else poison128(c.rs, l16, lb + c.ko + c.w * 1024 + (n - 5) * 4096);
}
DI int poison_mix(const Ctx& c, int lane) {
  int a = c.xo + (c.w & 1) * 2304 + 2048, b = c.oo + 1024 - 512, d = c.oo + 1024 + 20480 - 768;
  OPQ(a); OPQ(b); OPQ(d);
  return lane * 16 + (lane < 32 ? a : (lane < 48 ? b : d));
}
DI void publish_x(const Ctx& c, int lb, int xov, const unsigned* xp) {
  const int xos = lb + c.xo;
  OPQV(xov);
#pragma unroll
  for (int ct = 0; ct < 5; ++ct) st64(c.rs, ct ? xov + (ct - 1) * 1024 : xov, ct ? xos + 1024 : xos, (v2u){xp[2 * ct], xp[2 * ct + 1]});
}

template <bool ISB>
DI void layer(const Ctx& c, const Params& p, const int lyr, unsigned* xp, v4u* nxt) {
  int tid = c.tid; OPQV(tid);
  const int lane = tid & 63, w = c.w, t32 = lane & 31, hh = lane >> 5, t16 = lane & 15, g = lane >> 4;
  constexpr int NQ = ISB ? 10 : 5, NP = 2 * NQ * 21, NKS = ISB ? 5 : 3, TRS = ISB ? 160 : 96;
  constexpr int type = ISB ? 1 : 0;
  LAS char* L = c.L;
  int l16 = lane * 16;
  LAS char* wa = L + l16;
  LAS char* sd = wa + w * 25600;
  const int lb = __builtin_amdgcn_readfirstlane(lyr * LSZ);
  LSTAMP(0);
  v4u wst[25], up[13], xf[40];
  int wos = OFF_WO + (type * 8 + c.cs) * WO_SZ + w * 25600;
  int wts = OFF_WT + ((1 - type) * 16 + c.pj) * WT_SZ + w * 25600, wtu = OFF_WT + ((1 - type) * 16 + c.pj) * WT_SZ + 102400 + w * 12800;
  if (lyr == 3) { wts = OOR; wtu = OOR; }
  OPQ(wos); OPQ(wts); OPQ(wtu);
  constexpr int W1 = 10, XP = 20;
  auto bg = [&](int n) AI {
    if (n < 25 - W1) wst[W1 + n] = ldi(c.rs, l16, wos, (W1 + n) * 1024);
    else if (n < 38 - W1) up[n - (25 - W1)] = ldi(c.rs, l16, wtu, (n - (25 - W1)) * 1024);
    else if (n < 63 - W1) nxt[n - (38 - W1)] = ldi(c.rs, l16, wts, (n - (38 - W1)) * 1024);
  };
  const int xs = lb + R_X + (c.pi * 4 + w) * 40960;
  bar();
  {
    float s = 0.f, m = 0.f; f32x2 q2 = {0.f, 0.f};
    sfor<0, W1>([&](auto K) AI {
      constexpr int k = decltype(K)::v;
      stw(sd + k * 1024, nxt[k]); wst[k] = ldi(c.rs, l16, wos, k * 1024);
      if constexpr (k < 2) {
#pragma unroll
        for (int i = 5 * k; i < 5 * k + 5; ++i) s = sum2(xp[i], s);
        OPQV(s);
      }
      if constexpr (k == 2) { s = xsum32(xsum16(s)); m = s * (1.f / 80.f); OPQV(s); OPQV(m); }
      if constexpr (k >= 3 && k < 8) {
#pragma unroll
        for (int i = 2 * (k - 3); i < 2 * (k - 3) + 2; ++i) { const f32x2 d = (f32x2){blo(xp[i]), bhi(xp[i])} - m; q2 = __builtin_elementwise_fma(d, d, q2); }
        OPQV(q2);
      }
      if constexpr (k == 8) st64(c.rs, g == 0 ? t16 * 8 : OOR, lb + c.ko + w * 128, (v2u){f2u(s), f2u(xsum32(xsum16(q2[0] + q2[1])))});
      SB();
    });
  }
  VMWAIT(W1 + 1);
  LSTAMP(1);
#pragma unroll
  for (int k = 0; k < XP; ++k) { xf[k] = ldi(c.rs, l16, xs, k * 1024); SB(); if (k >= 4 && W1 + k - 4 < 25) stw(sd + (W1 + k - 4) * 1024, nxt[W1 + k - 4]); SB(); }
  v2u stv[4];
  int sv = hh * (4 * 16 * KVSLOT) + t32 * 8; const int ss = lb + R_KV + ((w >> 1) * 128 + c.pi) * KVSLOT + (w & 1) * 256;
  const int q4 = (lane & 15) >> 2, p4 = lane & 3;
  LAS char* wb = wa + L_T1;
  LAS char* w2 = L + L_T2 + hh * 384 + (t32 < 24 ? t32 : (t32 < 28 ? t32 - 4 : t32 - 12)) * 16;
  LAS char* csa = L + (hh ? L_ZERO : L_CS + type * 1024 + t32 * 32);
  constexpr int VO = ISB ? 80 : 48;
  LAS char* dst = L + (hh ? L_T1 : L_T0) + (32 * w + t32) * TRS;
  LAS char* ta = L + ((w & 1) ? L_T1 : L_T0) + (4 * g + q4) * TRS + 8 * p4;
  LAS char* tb = ta + VO + 32 * (w >> 1) - (((w >> 1) && p4 >= 2) ? 16 : 0);
  int kvv = 16 * (w >> 1) + t16 < 21 ? g * 336 + (16 * (w >> 1) + t16) * 16 : OOR;
  int kvw = g < NQ % 4 ? kvv : OOR;
  int mixv = poison_mix(c, lane);
  OPQV(wb); OPQV(w2); OPQV(ta); OPQV(tb); OPQV(kvv); OPQV(kvw); OPQV(dst); OPQV(sv); OPQV(csa); OPQV(mixv);
  bar();
  LSTAMP(2);
  f32x16 a0 = {}, a1 = {}, a2 = {}, a3 = {};
  constexpr int NL = 20, P1 = 5;
  bf16x8 A0[3], A1[P1], A2[3];
#pragma unroll
  for (int s = 0; s < 3; ++s) { A0[s] = rd(wa, s * 1024); A1[s] = rd(wb, s * 1024); A2[s] = rd(w2, s * 768); }
  SB();
  float rs = 0.f, mean = 0.f, m2 = 0.f; v4u mneg = {0u, 0u, 0u, 0u};
  v4u cs01, cs23;
  const int pb = lyr < 3 ? lb + LSZ : OOR;
  unsigned mk = chk128<5>(xf);
  float t0 = 0.f, t1 = 0.f, d0 = 0.f, d1 = 0.f, d2 = 0.f, d3 = 0.f;
  auto after = [&](auto M, auto G, auto R) AI {
    constexpr int mi = decltype(M)::v, gq = decltype(G)::v, r = decltype(R)::v, X2 = 40 - XP;
    if constexpr (mi < 2 * X2) { if constexpr (!(mi & 1)) xf[XP + mi / 2] = ldi(c.rs, l16, xs, (XP + mi / 2) * 1024); }
    else if constexpr (mi < 2 * X2 + 4) stv[mi - 2 * X2] = ld64(c.rs, sv, ss + (mi - 2 * X2) * 16 * KVSLOT);
    else if constexpr (mi >= 44 && mi < (ISB ? 56 : 58) && (mi & 1) == 0) poison_piece(c, pb, (mi - 44) / 2, l16, mixv);
    else if constexpr (mi >= 58 && (mi - 58) % 3 == 0) bg((mi - 58) / 3);
    if constexpr (gq < 7 && r >= 1 && r <= 5) { mk = mx4(r == 5 ? 0u : mk, xf[5 * gq + 10 - r]); OPQV(mk); }
    if constexpr (mi == 70) { t0 = u2f(stv[0].x) + u2f(stv[1].x); t1 = u2f(stv[2].x) + u2f(stv[3].x); OPQV(t0); OPQV(t1); }
    if constexpr (mi == 71) { mean = t0 + t1; OPQV(mean); t0 = u2f(stv[0].y) + u2f(stv[1].y); t1 = u2f(stv[2].y) + u2f(stv[3].y); OPQV(t0); OPQV(t1); }
    if constexpr (mi == 72) { mean = xsum32(mean); OPQV(mean); }
    if constexpr (mi == 73) { mean *= -(1.f / 640.f); OPQV(mean); m2 = t0 + t1; OPQV(m2); }
    if constexpr (mi == 79) { cs01 = *(LAS v4u*)(csa); cs23 = *(LAS v4u*)(csa + 16); }
    if constexpr (mi == 80) { mneg.x = pk2(mean, mean); mneg.y = mneg.x; OPQV(mneg); }
    if constexpr (mi == 81) { d0 = fmaf(u2f(stv[0].x), 1.f / 80.f, mean); d1 = fmaf(u2f(stv[1].x), 1.f / 80.f, mean); d2 = fmaf(u2f(stv[2].x), 1.f / 80.f, mean); OPQV(d0); OPQV(d1); OPQV(d2); }
    if constexpr (mi == 82) { d3 = fmaf(u2f(stv[3].x), 1.f / 80.f, mean); d0 *= d0; OPQV(d0); OPQV(d3); }
    if constexpr (mi == 83) { d0 = fmaf(d1, d1, d0); d0 = fmaf(d2, d2, d0); d0 = fmaf(d3, d3, d0); OPQV(d0); }
    if constexpr (mi == 90) { m2 = fmaf(d0, 80.f, m2); OPQV(m2); }
    if constexpr (mi == 91) { SB(); }
    if constexpr (mi == 92) { m2 = xsum32(m2); OPQV(m2); }
    if constexpr (mi == 93) { m2 = rbf(m2 * (1.f / 640.f)); OPQV(m2); }
    if constexpr (mi == 95) { m2 = rbf(m2 + 1e-5f); OPQV(m2); }
    if constexpr (mi == 96) { m2 = __builtin_amdgcn_sqrtf(m2); OPQV(m2); }
    if constexpr (mi == 98) { rs = __builtin_amdgcn_rcpf(rbf(m2)); OPQV(rs); }
    SB();
  };
  sfor<0, 40>([&](auto S) AI {
    constexpr int s = decltype(S)::v, gq = s / 5, u = s % 5;
    constexpr int per = s < 40 - NL ? 3 : 2;
    constexpr int m0 = s < 40 - NL ? 3 * s : 3 * (40 - NL) + 2 * (s - (40 - NL));
    if constexpr (u == 0) { retry128<5>(c, xf + 5 * gq, mk, l16, xs, 5 * gq * 1024, 1024, 0); SB(); }
    MF32(a0, A0[s % 3], asb(xf[s]));
    if constexpr (s + 3 < 40) A0[s % 3] = rd(wa, (s + 3) * 1024);
    after(IC<m0>{}, IC<gq>{}, IC<(5 - u) * per - 1>{});
    if constexpr (s < 40 - NL) {
      MF32(a1, A1[s % P1], asb(xf[s]));
      A1[(s + 3) % P1] = rd(wb, (s + 3) * 1024);
      after(IC<m0 + 1>{}, IC<gq>{}, IC<(5 - u) * per - 2>{});
    }
    MF32(a2, A2[s % 3], asb(xf[s]));
    if constexpr (s + 3 < 40) A2[s % 3] = rd(w2, (s + 3) * 768);
    if constexpr (s >= 38) { constexpr int z = 40 - NL + 3 + (s - 38); A1[z % P1] = rd(wb, z * 1024); }
    after(IC<m0 + per - 1>{}, IC<gq>{}, IC<(4 - u) * per>{});
    if constexpr (s == 31) { MF32(a0, asb(cs01), asb(mneg)); MF32(a2, asb(cs23), asb(mneg)); }
    if constexpr (s == 19) __builtin_amdgcn_s_barrier();
    if constexpr (s == 24) {
      unsigned m = max(umax3(stv[0].y, stv[1].y, stv[2].y), stv[3].y);
      if (__builtin_expect(__any(m == POISON), 0)) {
        NOUNROLL for (unsigned spins = 0; spins < SPIN_CAP; ++spins) {
          int ss2 = ss; OPQ(ss2); RCOUNT(3);
#pragma unroll
          for (int q = 0; q < 4; ++q) stv[q] = ld64(c.rs, sv, ss2 + q * 16 * KVSLOT);
          m = max(max(stv[0].y, stv[1].y), max(stv[2].y, stv[3].y));
          if (!__any(m == POISON)) break;
          __builtin_amdgcn_s_sleep(2);
        }
      }
      SB();
    }
  });
  LSTAMP(3);
  const v4u mng2 = {0u, 0u, mneg.x, mneg.x};
  {
    Phi<ISB> ph;
    MF32(a1, asb(cs01), asb(mng2));
    sfor<0, NL>([&](auto Z) AI {
      constexpr int z = decltype(Z)::v, s = 40 - NL + z;
      MF32(a1, A1[s % P1], asb(xf[s]));
      if constexpr (s + P1 < 40) A1[s % P1] = rd(wb, (s + P1) * 1024);
      ph.template gap<z>(a0, a2, 0, rs);
      SB();
    });
    ph.template rest<NL>(a0, a2, 0, rs);
    PINA(a2);
    const unsigned v8 = pk2(a2[4] * rs, a2[5] * rs), v9 = pk2(a2[6] * rs, a2[7] * rs);
#pragma unroll
    for (int i = 0; i < (ISB ? 5 : 2); ++i) *(LAS v4u*)(dst + 16 * i) = (v4u){ph.o[4 * i], ph.o[4 * i + 1], ph.o[4 * i + 2], ph.o[4 * i + 3]};
    if (!ISB) *(LAS v2u*)(dst + 32) = (v2u){ph.o[8], ph.o[9]};
    *(LAS v4u*)(dst + VO + 32) = (v4u){v8, v9, 0x00003F80u, 0u};
    if (!ISB) { SB(); }
    PINA(a1);
    unsigned v[8];
#pragma unroll
    for (int i = 0; i < 8; ++i) v[i] = pk2(a1[2 * i] * rs, a1[2 * i + 1] * rs);
    *(LAS v4u*)(dst + VO) = (v4u){v[0], v[1], v[2], v[3]}; *(LAS v4u*)(dst + VO + 16) = (v4u){v[4], v[5], v[6], v[7]};
  }
  SB();
  VMWAIT(14);
  bar();
  LSTAMP(4);
  LAS char* wc = wa + L_T3; OPQV(wc);
  constexpr int PD = 5;
  bf16x8 A3[PD];
  {
    constexpr int NF = ISB ? 3 : 2;
    bf16x8 fa[4][NF], fb[4];
    f32x4 kv[NF] = {};
#pragma unroll
    for (int ks = 0; ks < 6; ++ks) {
      if (ks < 4) {
        fb[ks] = tr2(tb, 32 * ks * TRS, (32 * ks + 16) * TRS);
#pragma unroll
        for (int ft = 0; ft < NF; ++ft) fa[ks][ft] = tr2(ta, 32 * ks * TRS + 32 * ft, (32 * ks + 16) * TRS + 32 * ft);
        SB();
      }
      if (ks >= 2) {
#pragma unroll
        for (int ft = NF - 1; ft >= 0; --ft) { kv[ft] = mfma16(fa[ks - 2][ft], fb[ks - 2], kv[ft]); PINA(kv[ft]); }
        SB();
      }
    }
    __builtin_amdgcn_s_barrier();
    const int kvs = lb + c.ko + 512 + (w & 1) * NQ * 336;
#pragma unroll
    for (int ft = NF - 1; ft >= 0; --ft) st128(c.rs, (ft < NF - 1 ? kvv : kvw) + ft * 1344, kvs, __builtin_bit_cast(v4u, kv[ft]));
    SB();
#pragma unroll
    for (int s = 0; s < PD; ++s) A3[s] = rd(wc, s * 1024);
  }
  SB();
  LSTAMP(5);
  constexpr int NR = ISB ? 2 : 1, PW = NP / 4 + (ISB ? 0 : 1);
  v4u kg[NR][16];
  int gv[NR], kvl[NR], pcs[NR], k0, k1, ov, xov;
  const int gs = lb + R_KV + c.pj * 16 * KVSLOT + 512;
  constexpr int GS = 26, GE = (40 - GS) / 2;
  MF32(a3, asb(cs23), asb(mng2));
  sfor<0, 40>([&](auto S) AI {
    constexpr int s = decltype(S)::v;
    MF32(a3, A3[s % PD], asb(xf[s]));
    if constexpr (s + PD < 40) A3[s % PD] = rd(wc, (s + PD) * 1024);
    if constexpr (s < 20 && (s & 1) == 0) bg(14 + (s >> 1));
    if constexpr (s % 3 == 1) stw(sd + (s / 3) * 1024, wst[s / 3]);
    if constexpr (s == 3 || (s == 5 && NR == 2)) {
      constexpr int r = (s - 3) / 2;
      const int pn = lane + 64 * r;
      pcs[r] = (pn < PW && w * PW + pn < NP) ? w * PW + pn : -1;
      OPQV(pcs[r]);
    }
    if constexpr (s == 9 || (s == 11 && NR == 2)) { constexpr int r = (s - 9) / 2; gv[r] = pcs[r] >= 0 ? pcs[r] * 16 : OOR; OPQV(gv[r]); }
    if constexpr (s == GS) VMWAIT(10);
    if constexpr (s >= GS && ((s - GS) & 1) == 0) { constexpr int n = (s - GS) >> 1; kg[n % NR][n / NR] = ld128(c.rs, gv[n % NR], gs + (n / NR) * KVSLOT); }
    SB();
  });
  LSTAMP(6);
  unsigned pq[NKS * 4];
  {
    Phi<ISB> ph;
    constexpr int NZ = ISB ? 13 : 12;
    sfor<0, NZ>([&](auto Z) AI {
      constexpr int z = decltype(Z)::v, n = GE + 2 * z;
      if constexpr (n < 16 * NR) kg[n % NR][n / NR] = ld128(c.rs, gv[n % NR], gs + (n / NR) * KVSLOT);
      if constexpr (z >= 1) { ph.template gap<2 * z - 2>(a3, a2, 8, rs); }
      if constexpr (n + 1 < 16 * NR) kg[(n + 1) % NR][(n + 1) / NR] = ld128(c.rs, gv[(n + 1) % NR], gs + ((n + 1) / NR) * KVSLOT);
      if constexpr (z < 12) stw(sd + (13 + z) * 1024, wst[13 + z]);
      if constexpr (z >= 1) { ph.template gap<2 * z - 1>(a3, a2, 8, rs); }
      SB();
    });
    ph.template rest<2 * NZ - 2>(a3, a2, 8, rs);
#pragma unroll
    for (int i = 0; i < (ISB ? 20 : 10); ++i) { pq[i] = ph.o[i]; OPQV(pq[i]); }
    if (!ISB) { pq[10] = 0u; pq[11] = 0u; }
#pragma unroll
    for (int r = 0; r < NR; ++r) {
      const int pc = pcs[r], hf = pc / 21, e = pc - hf * 21, head = hf / NQ, fq = hf - head * NQ;
      const int zi = w * 11 + lane - 53;
      kvl[r] = pc >= 0 ? L_KV + head * 2016 + e * 96 + fq * 8 : ((!ISB && lane >= 53 && zi < 42) ? L_KV + zi * 96 + 40 : L_DUMP);
      OPQV(kvl[r]);
    }
    const bool own = ((t32 >> 2) & 1) == hh;
    const int e0 = 4 * (t32 >> 3) + (t32 & 3), e1 = min(16 + e0, 20);
    k0 = own ? L_KV + hh * 2016 + e0 * 96 : L_ZERO; k1 = own ? L_KV + hh * 2016 + e1 * 96 : L_ZERO;
    ov = (t32 >> 4) * 20480 + t16 * 16 + 768 * hh; xov = ov - 760 * hh;
    OPQV(k0); OPQV(k1); OPQV(ov); OPQV(xov);
  }
  SB();
  LSTAMP(7);
  {
    f32x4 sum[NR];
#pragma unroll
    for (int i = 0; i < 16; i += 4) {
#pragma unroll
      for (int r = 0; r < NR; ++r) {
        retry128<4>(c, &kg[r][i], chk128<4>(&kg[r][i]), gv[r], gs, i * KVSLOT, KVSLOT, 1);
        const f32x4 t = (__builtin_bit_cast(f32x4, kg[r][i]) + __builtin_bit_cast(f32x4, kg[r][i + 1])) + (__builtin_bit_cast(f32x4, kg[r][i + 2]) + __builtin_bit_cast(f32x4, kg[r][i + 3]));
        sum[r] = i ? sum[r] + t : t;
        OPQV(sum[r]);
      }
    }
#pragma unroll
    for (int r = 0; r < NR; ++r) *(LAS v2u*)(L + kvl[r]) = (v2u){pk2(sum[r][0], sum[r][1]), pk2(sum[r][2], sum[r][3])};
  }
  SB();
  bg(24); bg(25);
  SB();
  bar();
  LSTAMP(8);
  {
    bf16x8 f0[NKS], f1[NKS];
#pragma unroll
    for (int ks = 0; ks < NKS; ++ks) f1[ks] = rd(L + k1, 16 * ks);
#pragma unroll
    for (int ks = 0; ks < NKS; ++ks) f0[ks] = rd(L + k0, 16 * ks);
    bg(26); bg(27);
    SB();
    f32x16 n0 = {}, n1 = {};
#pragma unroll
    for (int ks = 0; ks < NKS; ++ks) { MF32(n1, f1[ks], asb((v4u){pq[4 * ks], pq[4 * ks + 1], pq[4 * ks + 2], pq[4 * ks + 3]})); if (ks == 0 || ks == 2) bg(28 + ks / 2); SB(); }
    float id = 0.f; unsigned d8 = 0, d9 = 0;
    const int os = lb + c.oo;
#pragma unroll
    for (int ks = 0; ks < NKS; ++ks) {
      MF32(n0, f0[ks], asb((v4u){pq[4 * ks], pq[4 * ks + 1], pq[4 * ks + 2], pq[4 * ks + 3]}));
      if (ks == 0) bg(30);
      if (ks == NKS - 2) { PINA(n1); id = __builtin_amdgcn_rcpf(n1[4] + 1e-6f); OPQV(id); }
      if (ks == NKS - 1) { d8 = pk2(n1[0] * id, n1[1] * id); d9 = pk2(n1[2] * id, n1[3] * id); st64(c.rs, xov + 512, os, (v2u){d8, d9}); }
      SB();
    }
    PINA(n0);
    unsigned d[8];
#pragma unroll
    for (int i = 0; i < 8; ++i) d[i] = pk2(n0[2 * i] * id, n0[2 * i + 1] * id);
    st128(c.rs, ov, os, (v4u){d[0], d[1], d[2], d[3]});
    st128(c.rs, ov + 256, os, (v4u){d[4], d[5], d[6], d[7]});
  }
  SB();
  LSTAMP(9);
  constexpr int WPD = 40, HB = 10;
  bf16x8 wf[WPD];
  LAS char* wh = wa + 51200; OPQV(wh);
  LAS char* ud = wa + 102400 + w * 12800;
  LAS char* ul = hh ? L + L_DUMP : ud + 12 * 1024;
#pragma unroll
  for (int k = 0; k < 13; ++k) {
    if (k < HB) bg(31 + k);
    stw(k < 12 ? ud + k * 1024 : ul, up[k]);
    wf[k] = rd(wa, k * 1024);
    SB();
  }
  f32x4 acc[5];
#pragma unroll
  for (int ct = 0; ct < 5; ++ct) { acc[ct] = (f32x4){blo(xp[2 * ct]), bhi(xp[2 * ct]), blo(xp[2 * ct + 1]), bhi(xp[2 * ct + 1])}; PINA(acc[ct]); }
  xov = (g >> 1) * 512 + t16 * 16 + (g & 1) * 8; OPQV(xov);
  SB();
  VMWAIT(HB);
  LSTAMP(10);
  v4u of[20];
  const int oos = lb + R_O + (c.pi * 8 + 4 * c.th + w) * 20480;
#pragma unroll
  for (int s = 0; s < 20; ++s) {
    of[s] = ldi(c.rs, l16, oos, s * 1024); SB();
#pragma unroll
    for (int n = 13 + 2 * s; n < 15 + 2 * s; ++n) if (n < WPD) wf[n] = rd(wa, n * 1024);
    SB();
  }
  mk = chk128<5>(of);
  sfor<0, 100>([&](auto N) AI {
    constexpr int n = decltype(N)::v, s = n / 5, ct = n % 5, gq = n / 25, u = n % 25;
    if constexpr (u == 0) { retry128<5>(c, of + 5 * gq, mk, l16, oos, 5 * gq * 1024, 1024, 2); SB(); }
    acc[ct] = mfma16(wf[n % WPD], asb(of[s]), acc[ct]); PINA(acc[ct]); SB();
    if constexpr (n % 3 != 2) { constexpr int m = WPD + 2 * (n / 3) + n % 3; if constexpr (m < 100) wf[m % WPD] = (m < 50) ? rd(wa, m * 1024) : rd(wh, (m - 50) * 1024); }
    else if constexpr (gq < 3 && u >= 9 && u < 24) { constexpr int q = (u - 9) / 3; mk = mx4(q == 0 ? 0u : mk, of[5 * gq + 5 + q]); OPQV(mk); }
    else if constexpr (n >= 26) { constexpr int b = (n < 50 ? (n - 26) / 3 : (n < 75 ? 3 + (n - 50) / 3 : 6 + (n - 75) / 3)); bg(31 + HB + b); }
    SB();
  });
  LSTAMP(11);
#pragma unroll
  for (int ct = 0; ct < 5; ++ct) { xp[2 * ct] = pk2(acc[ct][0], acc[ct][1]); xp[2 * ct + 1] = pk2(acc[ct][2], acc[ct][3]); }
  if (lyr < 3) {
    publish_x(c, lb + LSZ, xov, xp);
  } else {
    unsigned short* o = p.out + (size_t)(128 * c.pi + 64 * c.th + 16 * w + t16) * 640 + 80 * c.cs + 4 * g;
#pragma unroll
    for (int ct = 0; ct < 5; ++ct) *(v2u*)(o + 16 * ct) = (v2u){xp[2 * ct], xp[2 * ct + 1]};
  }
  SB();
}

__global__ void __launch_bounds__(256, 1) mega(Params p) {
  extern __shared__ __attribute__((aligned(16))) char smem[];
  Ctx c;
  c.L = (LAS char*)smem;
  c.rs = __builtin_amdgcn_make_buffer_rsrc(p.ws, 0, WS_USED, 0x00020000);
  const int b = blockIdx.x, xcd = b & 7, r = b >> 3;
  c.pi = 4 * (xcd >> 1) + (r & 3); c.pj = 8 * (xcd & 1) + (r >> 2); c.th = c.pj >> 3; c.cs = c.pj & 7;
  c.tid = threadIdx.x; c.w = __builtin_amdgcn_readfirstlane(threadIdx.x >> 6);
  c.xo = R_X + (4 * c.pi + 2 * c.th + (c.w >> 1)) * 40960 + 5 * c.cs * 1024 + (c.w & 1) * 256;
  c.oo = R_O + (c.pi * 8 + 2 * c.w) * 20480 + 5 * c.pj * 256;
  c.ko = R_KV + (c.pj * 16 + c.pi) * KVSLOT;
  OPQ(c.xo); OPQ(c.oo); OPQ(c.ko);
  for (int i_ = threadIdx.x; i_ < 512; i_ += 256) { ZST(i_); } STAMP(0); CSTAMP(120);
  unsigned xp[10];
  v4u nxt[25];
  v2u ep;
  {
    LAS char* L = c.L;
    const int tid = c.tid, lane = tid & 63, w = c.w, t16 = lane & 15, g = lane >> 4, q4 = t16 >> 2, p4 = lane & 3;
    int l16 = lane * 16;
    constexpr int S_A = 0, S_B = 12288, S_WO = 24576, S_PHI = 36864;
    unsigned pf;
    {
      unsigned long long pc; asm volatile("s_getpc_b64 %0" : "=s"(pc));
      pf = *(const GAS unsigned*)((pc & ~127ull) + (unsigned)(((r & 3) * 64 + lane) * 128));
    }
    const rsrc_t ra = __builtin_amdgcn_make_buffer_rsrc((void*)(p.wqkv[0] + c.pi * 40 * 1920), 0, 40 * 3840, 0x00020000);
    const rsrc_t rb = __builtin_amdgcn_make_buffer_rsrc((void*)(p.wqkv[1] + c.pi * 40 * 1920), 0, 40 * 3840, 0x00020000);
    const rsrc_t ro = __builtin_amdgcn_make_buffer_rsrc((void*)((c.th ? p.wo[1] : p.wo[0]) + c.pi * 40 * 640), 0, 40 * 1280, 0x00020000);
    const rsrc_t rp = __builtin_amdgcn_make_buffer_rsrc((void*)p.wphi, 0, 32 * 800, 0x00020000);
    v4u ca[3], cb[3], co[3]; v2u cp[2];
    {
      const int row = tid >> 4;
      const int vq = t16 < 15 ? row * 3840 + t16 * 16 + (t16 >= 5 ? 1200 : 0) + (t16 >= 10 ? 1200 : 0) : OOR;
      const int vo = t16 < 10 ? row * 1280 + t16 * 16 : OOR;
      const int d = tid >> 3, q8 = tid & 7, vp = (d < 20 && q8 < 5) ? d * 40 + q8 * 8 : OOR;
#pragma unroll
      for (int k = 0; k < 3; ++k) cb[k] = __builtin_amdgcn_raw_buffer_load_b128(rb, vq, c.pj * 80 + k * 16 * 3840, 0);
#pragma unroll
      for (int k = 0; k < 2; ++k) cp[k] = __builtin_amdgcn_raw_buffer_load_b64(rp, vp, (2 * c.pj + k) * 800, 0);
#pragma unroll
      for (int k = 0; k < 3; ++k) ca[k] = __builtin_amdgcn_raw_buffer_load_b128(ra, vq, c.pj * 80 + k * 16 * 3840, 0);
#pragma unroll
      for (int k = 0; k < 3; ++k) co[k] = __builtin_amdgcn_raw_buffer_load_b128(ro, vo, c.cs * 160 + k * 16 * 1280, 0);
      const unsigned short* xr = p.x + (size_t)(128 * c.pi + 64 * c.th + 16 * w + t16) * 640 + 80 * c.cs + 4 * g;
#pragma unroll
      for (int ct = 0; ct < 5; ++ct) { const v2u v = *(const GAS v2u*)(xr + 16 * ct); xp[2 * ct] = v.x; xp[2 * ct + 1] = v.y; }
    }
    ep = ld64(c.rs, 0, OFF_CTRL);
    SB();
    {
      const int mixv = poison_mix(c, lane);
#pragma unroll
      for (int n = 0; n < 6; ++n) poison_piece(c, 0, n, l16, mixv);
    }
    if (tid < 24) *(LAS unsigned*)(L + L_ZERO + 4 * tid) = 0u;
    SB();
    LAS char* lt = L + tid * 16;
#pragma unroll
    for (int k = 0; k < 3; ++k) *(LAS v4u*)(lt + S_B + k * 4096) = cb[k];
#pragma unroll
    for (int k = 0; k < 2; ++k) *(LAS v2u*)(L + S_PHI + k * 2048 + tid * 8) = cp[k];
#pragma unroll
    for (int k = 0; k < 3; ++k) *(LAS v4u*)(lt + S_A + k * 4096) = ca[k];
#pragma unroll
    for (int k = 0; k < 3; ++k) *(LAS v4u*)(lt + S_WO + k * 4096) = co[k];
    bar();
    STAMP(101);
    {
      const int head = w & 1, sec = w >> 1;
      LAS char* pa = L + S_PHI + head * 2048 + 64 * (8 * g + q4) + 8 * p4;
      const bf16x8 f0 = tr2(pa, 0, 256), f1 = tr2(pa, 32, 288);
      LAS char* pr = L + S_B + t16 * 256 + sec * 80 + head * 40;
      LAS char* pb = pr + (g < 3 ? g * 16 : 0); LAS char* pb2 = pb + (g == 2 ? 0 : 8);
      LAS char* po = g == 0 ? pr + 32 : L + L_DUMP - 8192;
      f32x4 o0[3], o1[3];
#pragma unroll
      for (int rt = 0; rt < 3; ++rt) {
        const v2u lo = *(LAS v2u*)(pb + rt * 4096), hi = *(LAS v2u*)(pb2 + rt * 4096);
        const bf16x8 bb = asb((v4u){lo.x, lo.y, hi.x, hi.y});
        o0[rt] = mfma16(f0, bb, (f32x4){}); o1[rt] = mfma16(f1, bb, (f32x4){});
      }
      SB();
#pragma unroll
      for (int rt = 0; rt < 3; ++rt) {
        *(LAS v2u*)(pr + 8 * g + rt * 4096) = (v2u){pk2(o0[rt][0], o0[rt][1]), pk2(o0[rt][2], o0[rt][3])};
        *(LAS v2u*)(po + rt * 4096) = (v2u){pk2(o1[rt][0], o1[rt][1]), pk2(o1[rt][2], o1[rt][3])};
      }
    }
    const int head = p4 & 1, sq = 2 * (g & 1) + (p4 >> 1), r32 = lane & 31;
    const int tpos = w == 2 ? (sq == 0 ? 80 : (sq == 1 ? 160 : 0)) + 32 + 8 * head : (w == 3 ? 0 : 80 + 80 * w) + 48 * head + 8 * sq;
    LAS char* pt = L + (8 * (g >> 1) + q4) * 256 + tpos;
    const int tv = w == 2 ? (r32 < 24 ? (g >> 1) * 384 + r32 * 16 : OOR) : l16, tv2 = g < 2 ? tv : OOR;
    const int tstep = w == 2 ? 768 : 1024, ts = OFF_WT + c.pj * WT_SZ + (w == 3 ? L_T3 : w * 40960) + c.pi * 5 * (tstep >> 1);
    auto retile = [&](int type) AI {
      float s = 0.f;
#pragma unroll
      for (int k = 0; k < 3; ++k) {
        const v4u f = __builtin_bit_cast(v4u, tr2(pt + type * S_B, k * 4096, k * 4096 + 1024));
        st128r(c.rs, k < 2 ? tv : tv2, ts + type * 16 * WT_SZ + k * tstep, f);
        s = sum2(f.x, s); s = sum2(f.y, s); s = sum2(f.z, s); s = sum2(f.w, s);
      }
      s = xsum32(s);
      __builtin_amdgcn_raw_buffer_store_b32(f2u(s), c.rs, g < 2 ? r32 * 16 : OOR, OFF_CS + (c.pj * 16 + c.pi) * 1024 + type * 512 + w * 4, SC1);
    };
    retile(0);
#pragma unroll
    for (int k = 0; k < 2; ++k) {
      const int gi = 16 * k + 4 * w + g, gc = min(gi, 24), rg = gc / 5, ct = gc - rg * 5, RG = 5 * c.pi + rg;
      const bf16x8 f = tr2(L + S_WO + (8 * rg + q4) * 256 + 32 * ct + 8 * p4, 0, 1024);
      st128r(c.rs, gi < 25 ? (((RG >> 2) * 5 + ct) * 64 + (RG & 3) * 16 + t16) * 16 : OOR, OFF_WO + c.pj * WO_SZ, __builtin_bit_cast(v4u, f));
    }
    bar();
    retile(1);
    ep.x += 1u; ep.y += (ep.x == 0u);
    asm volatile("" :: "v"(pf));
    DRAIN();
    bar();
    STAMP(102);
    if (w == 0) {
      int f0 = 4096 + c.pi * 128, f1 = 8192 + c.pj * 128, f2 = 8192 + (c.pj ^ 8) * 128, f3 = f0 + c.pj * 8, f4 = f1 + c.pi * 8;
      OPQ(f0); OPQ(f1); OPQ(f2); OPQ(f3); OPQ(f4);
      __builtin_amdgcn_raw_buffer_store_b64(ep, c.rs, lane == 0 ? f3 : (lane == 1 ? f4 : OOR), OFF_CTRL, SC1);
      const int fo = (g == 0 ? f0 : (g == 1 ? f1 : f2)) + t16 * 8;
      DRAIN();
      NOUNROLL for (unsigned spins = 0; spins < SPIN_CAP; ++spins) {
        const v2u f = ld64(c.rs, fo, OFF_CTRL);
        if (__all(f.x == ep.x && f.y == ep.y)) break;
        __builtin_amdgcn_s_sleep(4);
      }
    }
    bar();
    STAMP(103);
    publish_x(c, 0, (g >> 1) * 512 + t16 * 16 + (g & 1) * 8, xp);
    const int wts = OFF_WT + c.pj * WT_SZ + w * 25600, wtu = OFF_WT + c.pj * WT_SZ + 102400 + w * 12800;
    v4u up[13]; unsigned cs[16];
#pragma unroll
    for (int k = 0; k < 25; ++k) nxt[k] = ldi(c.rs, l16, wts, k * 1024);
#pragma unroll
    for (int k = 0; k < 13; ++k) up[k] = ldi(c.rs, l16, wtu, k * 1024);
#pragma unroll
    for (int i = 0; i < 16; ++i) cs[i] = __builtin_amdgcn_raw_buffer_load_b32(c.rs, tid * 4 + (i & 3) * 1024, OFF_CS + (c.pj * 16 + (i & 12)) * 1024, SC1);
    SB();
    LAS char* d = L + 102400 + w * 12800 + l16;
#pragma unroll
    for (int k = 0; k < 12; ++k) *(LAS v4u*)(d + k * 1024) = up[k];
    if (lane < 32) *(LAS v4u*)(d + 12 * 1024) = up[12];
    float s = 0.f;
#pragma unroll
    for (int i = 0; i < 16; ++i) s += u2f(cs[i]);
    const float hi = rbf(s), mid = rbf(s - hi), lo = rbf(s - hi - mid);
    *(LAS v2u*)(L + L_CS + tid * 8) = (v2u){pk2(hi, mid), pk2(lo, 0.f)};
  }
  for (int it = 0; it < 2; ++it) {
    layer<false>(c, p, 2 * it, xp, nxt);
    layer<true>(c, p, 2 * it + 1, xp, nxt);
  }
  if (b == 0 && c.w == 0) __builtin_amdgcn_raw_buffer_store_b64(ep, c.rs, c.tid == 0 ? 0 : OOR, OFF_CTRL, SC1);
  STAMP(100); CSTAMP(121); DUMP();
}

extern "C" void kernel_launch(void* const* d_in, const int* in_sizes, int n_in, void* d_out, int out_size, void* d_ws, size_t ws_size, hipStream_t stream) {
  Params p;
  p.x = (const unsigned short*)d_in[0];
  p.wqkv[0] = (const unsigned short*)d_in[1]; p.wo[0] = (const unsigned short*)d_in[2];
  p.wqkv[1] = (const unsigned short*)d_in[4]; p.wphi = (const unsigned short*)d_in[5]; p.wo[1] = (const unsigned short*)d_in[6];
  p.ws = (unsigned char*)d_ws; p.out = (unsigned short*)d_out;
  (void)hipFuncSetAttribute((const void*)mega, hipFuncAttributeMaxDynamicSharedMemorySize, LDS_BYTES);
  mega<<<256, 256, LDS_BYTES, stream>>>(p);
}
```

```cpp
#include <hip/hip_runtime.h>
#include <hip/hip_bf16.h>
#include <stdint.h>

typedef unsigned v4u __attribute__((ext_vector_type(4)));
typedef unsigned v2u __attribute__((ext_vector_type(2)));
typedef short bf16x8 __attribute__((ext_vector_type(8)));
typedef short s16x4 __attribute__((ext_vector_type(4)));
typedef float f32x2 __attribute__((ext_vector_type(2)));
typedef float f32x4 __attribute__((ext_vector_type(4)));
typedef float f32x16 __attribute__((ext_vector_type(16)));
typedef __bf16 bf16x2v __attribute__((ext_vector_type(2)));
#define LAS __attribute__((address_space(3)))
#define GAS __attribute__((address_space(1)))
#define DI __device__ __forceinline__
#define AI __attribute__((always_inline))
typedef __amdgpu_buffer_rsrc_t rsrc_t;

constexpr int SC1 = 16;
constexpr unsigned POISON = 0xFFFFFFFFu;
constexpr unsigned SPIN_CAP = 1u << 16;
constexpr int OOR = 0x7FFF0000;

constexpr int XB_SZ = 2048 * 640 * 2;
constexpr int KVSLOT = 8192;
constexpr int R_X = 0, R_O = XB_SZ, R_KV = 2 * XB_SZ;
constexpr int LSZ = 2 * XB_SZ + 256 * KVSLOT;
constexpr int WT_SZ = 153600, WO_SZ = 102400;
constexpr int OFF_WT = 4 * LSZ;
constexpr int OFF_CS = OFF_WT + 2 * 16 * WT_SZ;
constexpr int OFF_WO = OFF_CS + 32 * 10240;
constexpr int OFF_HB = OFF_WO + 2 * 8 * WO_SZ;
constexpr int OFF_DBG = OFF_HB + 32768;
constexpr int WS_USED = OFF_DBG + (1 << 20);

constexpr int L_T0 = 0, L_T1 = 40960, L_T2 = 81920, L_T3 = 112640, L_MISC = 153600;
constexpr int L_KV = L_MISC;
constexpr int L_ZERO = L_KV + 4032;
constexpr int L_DUMP = L_ZERO + 96;
constexpr int L_CS = L_DUMP + 16;
constexpr int L_ST = L_CS + 2048;
constexpr int L_GO = L_ST + 1024;
constexpr int LDS_BYTES = 163840;

#define OPQ(x) do { x = __builtin_amdgcn_readfirstlane(x); asm volatile("" : "+s"(x)); } while (0)
#define OPQV(x) asm volatile("" : "+v"(x))
#define PINA(x) OPQV(x)
#define SB() __builtin_amdgcn_sched_barrier(0)
#define DRAIN() asm volatile("s_waitcnt vmcnt(0)" ::: "memory")
#define VMWAIT(n) asm volatile("s_waitcnt vmcnt(%0)" :: "i"(n) : "memory")
#define NOUNROLL _Pragma("nounroll")
DI v4u ld128(rsrc_t rs, int voff, int soff) { return __builtin_amdgcn_raw_buffer_load_b128(rs, voff, soff, SC1); }
DI v2u ld64(rsrc_t rs, int voff, int soff) { return __builtin_amdgcn_raw_buffer_load_b64(rs, voff, soff, SC1); }
DI v4u ldi(rsrc_t rs, int& voff, int soff, int coff) { OPQV(voff); return ld128(rs, voff + (coff & 4095), soff + (coff & ~4095)); }
DI unsigned np(unsigned u) { return u < 0xFFFFFFFEu ? u : 0xFFFFFFFEu; }
DI void st128r(rsrc_t rs, int voff, int soff, v4u v) { __builtin_amdgcn_raw_buffer_store_b128(v, rs, voff, soff, SC1); asm volatile("s_nop 1" :: "v"(v)); }
DI void st128(rsrc_t rs, int voff, int soff, v4u v) { st128r(rs, voff, soff, (v4u){v.x, np(v.y), v.z, np(v.w)}); }
DI void st64(rsrc_t rs, int voff, int soff, v2u v) { v2u q = {v.x, np(v.y)}; __builtin_amdgcn_raw_buffer_store_b64(q, rs, voff, soff, SC1); }
DI void stp128(rsrc_t rs, int voff, int soff, v4u v) { __builtin_amdgcn_raw_buffer_store_b128(v, rs, voff, soff, 0); asm volatile("s_nop 1" :: "v"(v)); }
DI void poison128(rsrc_t rs, int voff, int soff) { st128r(rs, voff, soff, (v4u){POISON, POISON, POISON, POISON}); }
DI unsigned pk2(float a, float b) { f32x2 v = {a, b}; return __builtin_bit_cast(unsigned, __builtin_convertvector(v, bf16x2v)); }
DI float u2f(unsigned u) { return __builtin_bit_cast(float, u); }
DI unsigned f2u(float f) { return __builtin_bit_cast(unsigned, f); }
DI float rbf(float a) { return u2f(pk2(a, a) & 0xFFFF0000u); }
DI float blo(unsigned u) { return u2f(u << 16); }
DI float bhi(unsigned u) { return u2f(u & 0xFFFF0000u); }
DI float sum2(unsigned u, float acc) { return __builtin_amdgcn_fdot2_f32_bf16(__builtin_bit_cast(bf16x2v, u), __builtin_bit_cast(bf16x2v, 0x3F803F80u), acc, false); }
DI unsigned umax3(unsigned a, unsigned b, unsigned c) { return max(max(a, b), c); }
DI unsigned mx4(unsigned m, v4u v) { return umax3(m, v.y, v.w); }
DI bf16x8 tr2(LAS char* p, int off1, int off2) {
  s16x4 a = __builtin_amdgcn_ds_read_tr16_b64_v4i16((LAS s16x4*)(p + off1));
  s16x4 b = __builtin_amdgcn_ds_read_tr16_b64_v4i16((LAS s16x4*)(p + off2));
  return __builtin_shufflevector(a, b, 0, 1, 2, 3, 4, 5, 6, 7);
}
DI bf16x8 rd(LAS char* p, int off) { return *(LAS bf16x8*)(p + off); }
DI f32x16 mfma32(bf16x8 a, bf16x8 b, f32x16 c) { return __builtin_amdgcn_mfma_f32_32x32x16_bf16(a, b, c, 0, 0, 0); }
DI f32x4 mfma16(bf16x8 a, bf16x8 b, f32x4 c) { return __builtin_amdgcn_mfma_f32_16x16x32_bf16(a, b, c, 0, 0, 0); }
DI bf16x8 asb(v4u v) { return __builtin_bit_cast(bf16x8, v); }
DI float xsum32(float v) { unsigned u = f2u(v); auto r = __builtin_amdgcn_permlane32_swap(u, u, false, false); return u2f(r[0]) + u2f(r[1]); }
DI float xsum16(float v) { unsigned u = f2u(v); auto r = __builtin_amdgcn_permlane16_swap(u, u, false, false); return u2f(r[0]) + u2f(r[1]); }
DI int lane_id() { int l; asm volatile("v_mbcnt_lo_u32_b32 %0, -1, 0\n\tv_mbcnt_hi_u32_b32 %0, -1, %0" : "=v"(l)); return l; }
DI void bar() { asm volatile("s_waitcnt lgkmcnt(0)" ::: "memory"); __builtin_amdgcn_s_barrier(); asm volatile("" ::: "memory"); }
#define MF32(acc, a, b) do { acc = mfma32(a, b, acc); PINA(acc); SB(); } while (0)
DI void stw(LAS char* p, v4u& v) { *(LAS v4u*)p = v; }
#define RAWBAR() do { SB(); __builtin_amdgcn_s_barrier(); SB(); } while (0)
template <int I> struct IC { static constexpr int v = I; };
template <int B, int E, class F> DI void sfor(F&& f) { if constexpr (B < E) { f(IC<B>{}); sfor<B + 1, E>(f); } }

#define STAMP(k) do {} while (0)
#define CL 2
#define CSTAMP(k) do {} while (0)
#define DUMP()
#define ZST(i)
#define RCOUNT(k)
#define LSTAMP(k) do { STAMP(1 + lyr * 24 + (k)); if ((k) >= 2 && (k) < 8 && lyr == CL) CSTAMP(112 + (k)); } while (0)

struct Params {
  const unsigned short* x; const unsigned short* wqkv[2]; const unsigned short* wo[2]; const unsigned short* wphi;
  unsigned char* ws; unsigned short* out;
};
struct Ctx { rsrc_t rs; LAS char* L; int pi, pj, th, cs, w, lane; int xo, oo, ko; };

template <int N> DI unsigned chk128(const v4u* x) { unsigned m = 0;
#pragma unroll
  for (int k = 0; k < N; ++k) m = mx4(m, x[k]);
  return m; }
template <int N, int G0, int G1>
DI void fixup(const Ctx& c, v4u* x, unsigned mk, int voff, int soff, int stride, int kind) {
  if (__builtin_expect(__any(mk == POISON), 0)) {
    NOUNROLL for (unsigned spins = 0; spins < SPIN_CAP; ++spins) {
      OPQ(soff);
      bool bad[G1 - G0];
#pragma unroll
      for (int q = G0; q < G1; ++q) bad[q - G0] = __any(chk128<N>(x + N * q) == POISON);
      SB();
#pragma unroll
      for (int q = G0; q < G1; ++q) {
        if (bad[q - G0]) {
#pragma unroll
          for (int k = N * q; k < N * q + N; ++k) x[k] = ld128(c.rs, voff, soff + k * stride);
        }
      }
      if (!bad[0]) break;
      RCOUNT(kind);
    }
    __builtin_amdgcn_s_waitcnt(0x0F70);
  }
}

DI float accv(const f32x16& m, const f32x16& t, int tb, int i) { return i < 16 ? m[i & 15] : t[tb + (i & 3)]; }
template <bool ISB> struct Phi;
template <> struct Phi<false> {
  static constexpr int NGAP = 24;
  unsigned o[10];
  float v[20], y[20], rl;
  template <int Z> DI void gap(f32x16& m, f32x16& t, int tb, float rs) {
    if constexpr (Z == 0) { rl = rs * 1.44269504088896f; OPQV(rl); }
    if constexpr (Z >= 1 && Z < 21) { OPQV(rl); y[Z - 1] = accv(m, t, tb, Z - 1) * rl; OPQV(y[Z - 1]); }
    if constexpr (Z >= 2 && Z < 22) { v[Z - 2] = __builtin_amdgcn_fmed3f(__builtin_amdgcn_exp2f(y[Z - 2]), 0.f, 1.f); OPQV(v[Z - 2]); y[Z - 2] = __builtin_amdgcn_fmed3f(y[Z - 2], 1.44269504e-6f, __builtin_inff()); OPQV(y[Z - 2]); }
    if constexpr (Z >= 3 && Z < 23) { v[Z - 3] = fmaf(y[Z - 3], 0.693147180559945f, v[Z - 3]); OPQV(v[Z - 3]); }
    if constexpr (Z >= 5 && Z < 24 && (Z & 1)) { o[(Z - 5) / 2] = pk2(v[Z - 5], v[Z - 4]); OPQV(o[(Z - 5) / 2]); }
  }
  template <int Z> DI void rest(f32x16& m, f32x16& t, int tb, float rs) { if constexpr (Z < NGAP) { gap<Z>(m, t, tb, rs); rest<Z + 1>(m, t, tb, rs); } }
};
template <> struct Phi<true> {
  unsigned o[20];
  f32x2 v[10], en[10], sp, sn; float mx, mn, rl;
  static constexpr int E0 = 8;
  template <int Z> DI void gap(f32x16& m, f32x16& t, int tb, float rs) {
    if constexpr (Z == 0) { rl = rs * 1.44269504088896f; OPQV(rl); mx = -__builtin_inff(); mn = __builtin_inff(); OPQV(mx); OPQV(mn); }
    if constexpr (Z >= 1 && Z < 6) {
      constexpr int b = 4 * (Z - 1);
      mx = fmaxf(fmaxf(mx, accv(m, t, tb, b)), accv(m, t, tb, b + 1)); OPQV(mx); mn = fminf(fminf(mn, accv(m, t, tb, b)), accv(m, t, tb, b + 1)); OPQV(mn);
      mx = fmaxf(fmaxf(mx, accv(m, t, tb, b + 2)), accv(m, t, tb, b + 3)); OPQV(mx); mn = fminf(fminf(mn, accv(m, t, tb, b + 2)), accv(m, t, tb, b + 3)); OPQV(mn);
    }
    if constexpr (Z == 6) { mx = -mx * rl; mn = mn * rl; OPQV(mx); OPQV(mn); }
    if constexpr (Z >= E0 - 1 && ((Z - E0 + 1) & 1) == 0 && (Z - E0 + 1) / 2 < 10) { constexpr int k = (Z - E0 + 1) / 2; OPQV(rl); arg(m, t, tb, k); OPQV(en[k]); OPQV(v[k]); }
    if constexpr (Z >= E0 && (Z - E0) / 2 < 10) {
      constexpr int k = (Z - E0) / 2;
      if constexpr (((Z - E0) & 1) == 0) { v[k] = ex(v[k]); OPQV(v[k]); } else { en[k] = ex(en[k]); OPQV(en[k]); }
    }
  }
  DI void arg(f32x16& m, f32x16& t, int tb, int k) { const f32x2 r2 = {rl, rl}, a = {accv(m, t, tb, 2 * k), accv(m, t, tb, 2 * k + 1)}; en[k] = __builtin_elementwise_fma(a, -r2, (f32x2){mn, mn}); v[k] = __builtin_elementwise_fma(a, r2, (f32x2){mx, mx}); }
  DI f32x2 ex(f32x2 a) { return (f32x2){__builtin_amdgcn_exp2f(a[0]), __builtin_amdgcn_exp2f(a[1])}; }
  template <int Z> DI void rest(f32x16& m, f32x16& t, int tb, float rs) {
    OPQV(rl);
#pragma unroll
    for (int k = 0; k < 10; ++k) {
      if (E0 - 1 + 2 * k >= Z) arg(m, t, tb, k);
      if (E0 + 2 * k >= Z) v[k] = ex(v[k]);
      if (E0 + 2 * k + 1 >= Z) en[k] = ex(en[k]);
    }
#pragma unroll
    for (int k = 0; k < 10; ++k) { sp = k ? sp + v[k] : v[k]; sn = k ? sn + en[k] : en[k]; }
    const float ip = __builtin_amdgcn_rcpf(sp[0] + sp[1]), in = __builtin_amdgcn_rcpf(sn[0] + sn[1]);
#pragma unroll
    for (int k = 0; k < 10; ++k) { const f32x2 a2 = v[k] * ip, b2 = en[k] * in; o[k] = pk2(a2[0], a2[1]); o[10 + k] = pk2(b2[0], b2[1]); }
  }
};

DI void poison_all(const Ctx& c, int lb, int n, int l16, int lane) {
  int a = c.xo + (c.w & 1) * 2304 + 2048, b = c.oo + 1024 - 512, d = c.oo + 1024 + 20480 - 768;
  OPQ(a); OPQ(b); OPQ(d);
  poison128(c.rs, l16, lb + c.xo + (c.w & 1) * 2304); poison128(c.rs, l16 + 1024, lb + c.xo + (c.w & 1) * 2304);
  poison128(c.rs, l16, lb + c.oo); poison128(c.rs, l16, lb + c.oo + 20480);
  poison128(c.rs, l16 + (lane < 32 ? a : (lane < 48 ? b : d)), lb);
  poison128(c.rs, l16, lb + c.ko + c.w * 1024);
  if (n > 6) poison128(c.rs, l16, lb + c.ko + c.w * 1024 + 4096);
}
DI void publish_x(const Ctx& c, int lb, int xov, const unsigned* xp) {
  const int xos = lb + c.xo;
  OPQV(xov);
#pragma unroll
  for (int ct = 0; ct < 5; ++ct) st64(c.rs, ct ? xov + (ct - 1) * 1024 : xov, ct ? xos + 1024 : xos, (v2u){xp[2 * ct], xp[2 * ct + 1]});
}
DI void publish_stats(const Ctx& c, int lb, int lane, const unsigned* xp) {
  float s = 0.f;
#pragma unroll
  for (int i = 0; i < 10; ++i) s = sum2(xp[i], s);
  s = xsum32(xsum16(s));
  const float m = s * (1.f / 80.f);
  f32x2 q2 = {0.f, 0.f};
#pragma unroll
  for (int i = 0; i < 10; ++i) { const f32x2 d = (f32x2){blo(xp[i]), bhi(xp[i])} - m; q2 = __builtin_elementwise_fma(d, d, q2); }
  const float q = xsum32(xsum16(q2[0] + q2[1]));
  st64(c.rs, lane < 16 ? lane * 8 : OOR, lb + c.ko + c.w * 128, (v2u){f2u(s), f2u(q)});
}
#define HINT 0
#define PD3 0
#define PS 1
#define PD1 4
#define PD2 4
#define B4S 29
template <bool ISB>
DI void layer_c(const Ctx& c, const Params& p, const int lyr, unsigned* xp) {
  int lane = lane_id();
  const int w = c.w, t32 = lane & 31, hh = lane >> 5, t16 = lane & 15, g = lane >> 4;
  constexpr int NQ = ISB ? 10 : 5, NKS = ISB ? 5 : 3, TRS = ISB ? 160 : 96;
  constexpr int type = ISB ? 1 : 0;
  LAS char* L = c.L;
  int l16 = lane * 16;
  LAS char* wa = L + l16;
  const int lb = __builtin_amdgcn_readfirstlane(lyr * LSZ);
  v4u xf[40];
  const int xs = lb + R_X + (c.pi * 4 + w) * 40960;
  const int q4 = (lane & 15) >> 2, p4 = lane & 3;
  LAS char* wb = wa + L_T1;
  LAS char* wc = wa + L_T3;
  LAS char* w2 = L + L_T2 + hh * 384 + (t32 < 24 ? t32 : (t32 < 28 ? t32 - 4 : t32 - 12)) * 16;
  OPQV(wb); OPQV(wc); OPQV(w2);
  SB();
  if (ISB || lyr) { VMWAIT(1); __builtin_amdgcn_s_sleep(PD1); }
  LSTAMP(1);
  constexpr int XP = 16, XW = 26;
#pragma unroll
  for (int k = 0; k < XP; ++k) { xf[k] = ldi(c.rs, l16, xs, k * 1024); SB(); }
  RAWBAR();
  LSTAMP(2);
  f32x16 a0 = {}, a1 = {}, a2 = {}, a3 = {};
  constexpr int P1 = 4;
  bf16x8 A0[3], A1[P1], A2[3], A3[P1];
#pragma unroll
  for (int s = 0; s < 2; ++s) { A0[s] = rd(wa, s * 1024); A2[s] = rd(w2, s * 768); A1[s] = rd(wb, s * 1024); A3[s] = rd(wc, s * 1024); }
  SB();
  float rs = 0.f; v4u mneg = {0u, 0u, 0u, 0u}, cs01, cs23; v2u stt;
  LAS char* dst; LAS char* ta; LAS char* tb; LAS char* csa; int kvv, kvw;
  constexpr int VO = ISB ? 80 : 48;
  unsigned mk = chk128<5>(xf);
  auto after = [&](auto M, auto G, auto R) AI {
    constexpr int mi = decltype(M)::v, gq = decltype(G)::v, r = decltype(R)::v;
    if constexpr (mi < 2 * (XW - XP)) { if constexpr (mi & 1) xf[XP + mi / 2] = ldi(c.rs, l16, xs, (XP + mi / 2) * 1024); }
    else if constexpr (mi < 36) { if constexpr (mi & 1) { constexpr int k = XP + mi / 2; static_assert(4 * (k - XW) + 3 <= mi, "window"); xf[k] = ldi(c.rs, l16, xs, k * 1024); } }
    else if constexpr ((mi & 3) == 3 && XW + mi / 4 - 1 < 40) { constexpr int k = XW + mi / 4 - 1; xf[k] = ldi(c.rs, l16, xs, k * 1024); }
    if constexpr (gq < 7 && r >= 1 && r <= 5) { mk = mx4(r == 5 ? 0u : mk, xf[5 * gq + 10 - r]); OPQV(mk); }
    if constexpr (mi == 60) { dst = L + (hh ? L_T1 : L_T0) + (32 * w + t32) * TRS; OPQV(dst); }
    if constexpr (mi == 62) { ta = L + ((w & 1) ? L_T1 : L_T0) + (4 * g + q4) * TRS + 8 * p4; OPQV(ta); }
    if constexpr (mi == 64) { tb = ta + VO + 32 * (w >> 1) - (((w >> 1) && p4 >= 2) ? 16 : 0); OPQV(tb); }
    if constexpr (mi == 66) { kvv = g * 336 + (16 * (w >> 1) + t16) * 16; OPQV(kvv); }
    if constexpr (mi == 68) { kvv = 16 * (w >> 1) + t16 < 21 ? kvv : OOR; OPQV(kvv); }
    if constexpr (mi == 70) { kvw = g < NQ % 4 ? kvv : OOR; OPQV(kvw); }
    if constexpr (mi == 72) { csa = L + (hh ? L_ZERO : L_CS + type * 1024 + t32 * 32); OPQV(csa); }
    if constexpr (mi == 100) { cs01 = *(LAS v4u*)(csa); cs23 = *(LAS v4u*)(csa + 16); stt = *(LAS v2u*)(L + L_ST + w * 256 + t32 * 8); }
    if constexpr (mi == 103) { mneg.x = stt.x; mneg.y = stt.x; rs = u2f(stt.y); OPQV(mneg); OPQV(rs); }
    SB();
  };
  sfor<0, 40>([&](auto S) AI {
    constexpr int s = decltype(S)::v, gq = s / 5, u = s % 5;
    constexpr int per = s < 20 ? 4 : 2, m0 = s < 20 ? 4 * s : 40 + 2 * s, pd = s < 19 ? 2 : 3;
    if constexpr (u == 0) { fixup<5, gq, (gq == 0 ? 3 : gq == 1 ? 5 : gq == 2 ? 7 : 8)>(c, xf, mk, l16, xs, 1024, 0); SB(); }
    MF32(a0, A0[s % 3], asb(xf[s]));
    if constexpr (s == 19) { A0[21 % 3] = rd(wa, 21 * 1024); A2[21 % 3] = rd(w2, 21 * 768); }
    if constexpr (s + pd < 40) A0[(s + pd) % 3] = rd(wa, (s + pd) * 1024);
    after(IC<m0>{}, IC<gq>{}, IC<(5 - u) * per - 1>{});
    MF32(a2, A2[s % 3], asb(xf[s]));
    if constexpr (s + pd < 40) A2[(s + pd) % 3] = rd(w2, (s + pd) * 768);
    if constexpr (s >= 36) A1[(s - 16) % P1] = rd(wb, (s - 16) * 1024);
    after(IC<m0 + 1>{}, IC<gq>{}, IC<(5 - u) * per - 2>{});
    if constexpr (s < 20) {
      MF32(a1, A1[s % 2], asb(xf[s]));
      if constexpr (s + 2 < 20) A1[s % 2] = rd(wb, (s + 2) * 1024);
      after(IC<m0 + 2>{}, IC<gq>{}, IC<(5 - u) * per - 3>{});
      MF32(a3, A3[s % 2], asb(xf[s]));
      if constexpr (s + 2 < 20) A3[s % 2] = rd(wc, (s + 2) * 1024);
      after(IC<m0 + 3>{}, IC<gq>{}, IC<(5 - u) * per - 4>{});
    }
    if constexpr (s == 33) { MF32(a0, asb(cs01), asb(mneg)); MF32(a2, asb(cs23), asb(mneg)); }
    if constexpr (s == B4S) RAWBAR();
  });
  LSTAMP(3);
  const v4u mng2 = {0u, 0u, mneg.x, mneg.x};
  {
    Phi<ISB> ph;
    MF32(a1, asb(cs01), asb(mng2));
    sfor<0, 20>([&](auto Z) AI {
      constexpr int z = decltype(Z)::v, s = 20 + z;
      MF32(a1, A1[s % P1], asb(xf[s]));
      if constexpr (s + P1 < 40) A1[s % P1] = rd(wb, (s + P1) * 1024);
      ph.template gap<z>(a0, a2, 0, rs);
      SB();
    });
    ph.template rest<20>(a0, a2, 0, rs);
    OPQV(rs);
    const unsigned v8 = pk2(a2[4] * rs, a2[5] * rs), v9 = pk2(a2[6] * rs, a2[7] * rs);
#pragma unroll
    for (int i = 0; i < (ISB ? 5 : 2); ++i) *(LAS v4u*)(dst + 16 * i) = (v4u){ph.o[4 * i], ph.o[4 * i + 1], ph.o[4 * i + 2], ph.o[4 * i + 3]};
    if (!ISB) *(LAS v2u*)(dst + 32) = (v2u){ph.o[8], ph.o[9]};
    *(LAS v4u*)(dst + VO + 32) = (v4u){v8, v9, 0x00003F80u, 0u};
    if (!ISB) { SB(); }
    unsigned v[8];
#pragma unroll
    for (int i = 0; i < 8; ++i) v[i] = pk2(a1[2 * i] * rs, a1[2 * i + 1] * rs);
    *(LAS v4u*)(dst + VO) = (v4u){v[0], v[1], v[2], v[3]}; *(LAS v4u*)(dst + VO + 16) = (v4u){v[4], v[5], v[6], v[7]};
  }
  SB();
  bar();
  LSTAMP(4);
  {
    constexpr int NF = ISB ? 3 : 2;
    bf16x8 fa[4][NF], fb[4];
    f32x4 kv[NF] = {};
#pragma unroll
    for (int ks = 0; ks < 6; ++ks) {
      if (ks < 4) {
        fb[ks] = tr2(tb, 32 * ks * TRS, (32 * ks + 16) * TRS);
#pragma unroll
        for (int ft = 0; ft < NF; ++ft) fa[ks][ft] = tr2(ta, 32 * ks * TRS + 32 * ft, (32 * ks + 16) * TRS + 32 * ft);
        SB();
      }
      if (ks >= 2) {
#pragma unroll
        for (int ft = NF - 1; ft >= 0; --ft) { kv[ft] = mfma16(fa[ks - 2][ft], fb[ks - 2], kv[ft]); OPQV(kv[ft]); }
        SB();
      }
    }
    const int kvs = lb + c.ko + 512 + (w & 1) * NQ * 336;
#pragma unroll
    for (int s = 0; s < P1; ++s) A3[(20 + s) % P1] = rd(wc, (20 + s) * 1024);
    SB();
#pragma unroll
    for (int ft = NF - 1; ft >= 0; --ft) st128(c.rs, (ft < NF - 1 ? kvv : kvw) + ft * 1344, kvs, __builtin_bit_cast(v4u, kv[ft]));
  }
  RAWBAR();
  LSTAMP(5);
  int k0, k1, ov, xov;
  unsigned pq[NKS * 4];
  MF32(a3, asb(cs23), asb(mng2));
  sfor<20, 40>([&](auto S) AI {
    constexpr int s = decltype(S)::v;
    MF32(a3, A3[s % P1], asb(xf[s]));
    if constexpr (s + P1 < 40) A3[s % P1] = rd(wc, (s + P1) * 1024);
    if constexpr (s == 21) { k0 = 4 * (t32 >> 3) + (t32 & 3); OPQV(k0); k1 = min(16 + k0, 20); OPQV(k1); }
    if constexpr (s == 23) { k0 = L_KV + hh * 2016 + k0 * 96; k1 = L_KV + hh * 2016 + k1 * 96; OPQV(k0); OPQV(k1); }
    if constexpr (s == 25) { const bool own = ((t32 >> 2) & 1) == hh; k0 = own ? k0 : L_ZERO; k1 = own ? k1 : L_ZERO; OPQV(k0); OPQV(k1); }
    if constexpr (s == 27) { xov = (t32 >> 4) * 20480 + t16 * 16; OPQV(xov); }
    if constexpr (s == 29) { ov = xov + 768 * hh; xov += 8 * hh; OPQV(ov); OPQV(xov); }
    SB();
  });
  LSTAMP(6);
  {
    Phi<ISB> ph;
    sfor<0, 8>([&](auto Z) AI { ph.template gap<decltype(Z)::v>(a3, a2, 8, rs); });
    ph.template rest<8>(a3, a2, 8, rs);
#pragma unroll
    for (int i = 0; i < (ISB ? 20 : 10); ++i) { pq[i] = ph.o[i]; OPQV(pq[i]); }
    if (!ISB) { pq[10] = 0u; pq[11] = 0u; }
  }
  f32x4 acc[5];
#pragma unroll
  for (int ct = 0; ct < 5; ++ct) { acc[ct] = (f32x4){blo(xp[2 * ct]), bhi(xp[2 * ct]), blo(xp[2 * ct + 1]), bhi(xp[2 * ct + 1])}; OPQV(acc[ct]); }
  LSTAMP(7);
  RAWBAR();
  LSTAMP(8);
  {
    bf16x8 f0[NKS], f1[NKS];
#pragma unroll
    for (int ks = 0; ks < NKS; ++ks) f1[ks] = rd(L + k1, 16 * ks);
#pragma unroll
    for (int ks = 0; ks < NKS; ++ks) f0[ks] = rd(L + k0, 16 * ks);
    SB();
    f32x16 n0 = {}, n1 = {};
#pragma unroll
    for (int ks = 0; ks < NKS; ++ks) MF32(n1, f1[ks], asb((v4u){pq[4 * ks], pq[4 * ks + 1], pq[4 * ks + 2], pq[4 * ks + 3]}));
    float id = 0.f;
    const int os = lb + c.oo;
#pragma unroll
    for (int ks = 0; ks < NKS; ++ks) {
      MF32(n0, f0[ks], asb((v4u){pq[4 * ks], pq[4 * ks + 1], pq[4 * ks + 2], pq[4 * ks + 3]}));
      if (ks == NKS - 2) { OPQV(n1); id = __builtin_amdgcn_rcpf(n1[4] + 1e-6f); OPQV(id); }
      if (ks == NKS - 1) st64(c.rs, xov + 512, os, (v2u){pk2(n1[0] * id, n1[1] * id), pk2(n1[2] * id, n1[3] * id)});
      SB();
    }
    unsigned d[8];
#pragma unroll
    for (int i = 0; i < 8; ++i) d[i] = pk2(n0[2 * i] * id, n0[2 * i + 1] * id);
    st128(c.rs, ov, os, (v4u){d[0], d[1], d[2], d[3]});
    st128(c.rs, ov + 256, os, (v4u){d[4], d[5], d[6], d[7]});
  }
  RAWBAR();
  LSTAMP(9);
  constexpr int WPD = 32;
  bf16x8 wf[WPD];
  LAS char* wh = wa + 51200; OPQV(wh);
#pragma unroll
  for (int k = 0; k < WPD; ++k) wf[k] = rd(wa, k * 1024);
  xov = (g >> 1) * 512 + t16 * 16 + (g & 1) * 8; OPQV(xov);
  SB();
  VMWAIT(0);
  __builtin_amdgcn_s_sleep(PD2);
  LSTAMP(10);
  v4u of[20];
  const int oos = lb + R_O + (c.pi * 8 + 4 * c.th + w) * 20480;
#pragma unroll
  for (int s = 0; s < 20; ++s) { of[s] = ldi(c.rs, l16, oos, s * 1024); SB(); }
  mk = chk128<5>(of);
  sfor<0, 100>([&](auto N) AI {
    constexpr int n = decltype(N)::v, s = n / 5, ct = n % 5, gq = n / 25, u = n % 25;
    if constexpr (u == 0) { fixup<5, gq, 4>(c, of, mk, l16, oos, 1024, 2); SB(); }
    acc[ct] = mfma16(wf[n % WPD], asb(of[s]), acc[ct]); OPQV(acc[ct]); SB();
    if constexpr (n % 4 != 3) { constexpr int m = WPD + 3 * (n / 4) + n % 4; if constexpr (m < 100) wf[m % WPD] = (m < 50) ? rd(wa, m * 1024) : rd(wh, (m - 50) * 1024); }
    if constexpr (gq < 3 && u >= 14 && u < 24 && (u & 1) == 0) { constexpr int q = (u - 14) / 2; mk = mx4(q == 0 ? 0u : mk, of[5 * gq + 5 + q]); OPQV(mk); }
    if constexpr (n == 96) bar();
    SB();
  });
  LSTAMP(11);
#pragma unroll
  for (int ct = 0; ct < 5; ++ct) { xp[2 * ct] = pk2(acc[ct][0], acc[ct][1]); xp[2 * ct + 1] = pk2(acc[ct][2], acc[ct][3]); }
  if (lyr < 3) {
    publish_x(c, lb + LSZ, xov, xp);
    SB();
    publish_stats(c, lb + LSZ, lane, xp);
  } else {
    unsigned short* o = p.out + (size_t)(128 * c.pi + 64 * c.th + 16 * w + t16) * 640 + 80 * c.cs + 4 * g;
#pragma unroll
    for (int ct = 0; ct < 5; ++ct) *(v2u*)(o + 16 * ct) = (v2u){xp[2 * ct], xp[2 * ct + 1]};
  }
  SB();
  LSTAMP(0);
}

#define KM 55
#define Q1 8
#define SL1 1
#define Q2 20
#define SL2 0
#define Q3 24
#define Q4 30
#define WE 14
#define Q5 40
#define HS 10
#define SL3 0
template <bool ISB>
DI void layer_h(const Ctx& c, const int lyr) {
  int lane = lane_id();
  const int w = c.w, t32 = lane & 31, hh = lane >> 5;
  constexpr int NQ = ISB ? 10 : 5, NP = 2 * NQ * 21;
  constexpr int type = ISB ? 1 : 0;
  LAS char* L = c.L;
  int l16 = lane * 16;
  LAS char* wa = L + l16;
  LAS char* sd = wa + w * 25600;
  const int lb = __builtin_amdgcn_readfirstlane(lyr * LSZ);
  int wos = OFF_WO + (type * 8 + c.cs) * WO_SZ + w * 25600;
  int wt0 = OFF_WT + ((1 - type) * 16 + c.pj) * WT_SZ;
  if (lyr == 3) wt0 = OOR - 0x1000000;
  OPQ(wos); OPQ(wt0);
  bar();
  {
    v2u stv[4];
    const int sv = hh * (4 * 16 * KVSLOT) + t32 * 8; int ss = lb + R_KV + ((w >> 1) * 128 + c.pi) * KVSLOT + (w & 1) * 256;
    NOUNROLL for (unsigned spins = 0; spins < SPIN_CAP; ++spins) {
#pragma unroll
      for (int q = 0; q < 4; ++q) stv[q] = ld64(c.rs, sv, ss + q * 16 * KVSLOT);
      if (!__any(max(umax3(stv[0].y, stv[1].y, stv[2].y), stv[3].y) == POISON)) break;
      RCOUNT(3);
      __builtin_amdgcn_s_sleep(4);
    }
    const float mean = xsum32((u2f(stv[0].x) + u2f(stv[1].x)) + (u2f(stv[2].x) + u2f(stv[3].x))) * (1.f / 640.f);
    float m2 = 0.f;
#pragma unroll
    for (int q = 0; q < 4; ++q) { const float d = u2f(stv[q].x) * (1.f / 80.f) - mean; m2 += fmaf(80.f * d, d, u2f(stv[q].y)); }
    const float var = rbf(xsum32(m2) * (1.f / 640.f));
    const float rs = __builtin_amdgcn_rcpf(rbf(__builtin_amdgcn_sqrtf(rbf(var + 1e-5f))));
    *(LAS v2u*)(L + L_ST + w * 256 + t32 * 8) = (v2u){pk2(-mean, -mean), f2u(rs)};
  }
  constexpr int NR = ISB ? 2 : 1, PW = NP / 4 + (ISB ? 0 : 1);
  int gv[NR], kvl[NR];
#pragma unroll
  for (int r = 0; r < NR; ++r) {
    const int pn = lane + 64 * r, pc = w * PW + pn; const bool ok = pn < PW && pc < NP;
    const int hf = (pc * 3121) >> 16, zi = w * 11 + lane - 53;
    gv[r] = ok ? pc * 16 : OOR;
    kvl[r] = ok ? L_KV + (pc - hf * 21) * 96 + hf * 8 + (hf >= NQ ? 2016 - NQ * 8 : 0) : ((!ISB && lane >= 53 && zi < 42) ? L_KV + zi * 96 + 40 : L_DUMP);
    OPQV(gv[r]); OPQV(kvl[r]);
  }
  v4u wst[25], up[13], nxt[25];
  auto bg = [&](int n) AI {
    if (n < 25) wst[n] = ldi(c.rs, l16, wos, n * 1024);
    else if (n < 38) up[n - 25] = ldi(c.rs, l16, wt0 + w * 12800, 102400 + (n - 25) * 1024);
    else if (n < 63) nxt[n - 38] = ldi(c.rs, l16, wt0 + w * 25600, (n - 38) * 1024);
    SB();
  };
#pragma unroll
  for (int n = 0; n < Q1; ++n) { bg(n); __builtin_amdgcn_s_sleep(SL1); }
  bar();
  const unsigned t4 = (unsigned)__builtin_amdgcn_s_memrealtime();
  const int hb = OFF_HB + lyr * 8192;
  if (w == 0) __builtin_amdgcn_raw_buffer_store_b64((v2u){t4, 0u}, c.rs, lane == 0 ? 0 : OOR, hb + (c.pi * 16 + c.pj) * 32, SC1);
  poison_all(c, lyr < 3 ? lb + LSZ : OOR, ISB ? 6 : 7, l16, lane);
#pragma unroll
  for (int n = Q1; n < Q2; ++n) { bg(n); __builtin_amdgcn_s_sleep(SL2); }
  VMWAIT(Q2 - Q1);
  RAWBAR();
#pragma unroll
  for (int n = Q2; n < Q3; ++n) bg(n);
  v2u hv[4];
  const int hvo = w == 3 ? lane * 32 : OOR;
#pragma unroll
  for (int q = 0; q < 4; ++q) hv[q] = ld64(c.rs, hvo, hb + q * 2048);
  RAWBAR();
  const unsigned t6 = (unsigned)__builtin_amdgcn_s_memrealtime();
  v4u kg[NR][16];
  const int gs = lb + R_KV + c.pj * 16 * KVSLOT + 512;
#pragma unroll
  for (int k = 0; k < WE; ++k) { if (Q3 + k < Q4) bg(Q3 + k); stw(sd + k * 1024, wst[k]); SB(); }
  {
    volatile LAS unsigned* go = (volatile LAS unsigned*)(L + L_GO) + (lyr & 1);
    int lag = 0;
    if (w == 3) {
      NOUNROLL for (unsigned spins = 0; spins < 64; ++spins) {
        bool ok = true; lag = 0;
#pragma unroll
        for (int q = 0; q < 4; ++q) { ok &= hv[q].y == 0u; lag = max(lag, (int)(hv[q].x - t4)); }
        if (__all(ok)) break;
        RCOUNT(5);
#pragma unroll
        for (int q = 0; q < 4; ++q) if (__any(hv[q].y != 0u)) hv[q] = ld64(c.rs, hvo, hb + q * 2048);
        lag = 0;
      }
#pragma unroll
      for (int d = 32; d >= 1; d >>= 1) lag = max(lag, __shfl_xor(lag, d));
      lag = min(lag, 400);
      *go = ((unsigned)(lyr + 1) << 16) | (unsigned)lag;
    } else {
      unsigned v = 0;
      NOUNROLL for (unsigned spins = 0; spins < 4096; ++spins) { v = *go; if ((v >> 16) == (unsigned)(lyr + 1)) break; __builtin_amdgcn_s_sleep(1); }
      lag = v & 0x1FF;
    }
    lag = __builtin_amdgcn_readfirstlane(lag) + KM;
    NOUNROLL while ((int)((unsigned)__builtin_amdgcn_s_memrealtime() - t6) < lag) __builtin_amdgcn_s_sleep(1);
  }
#pragma unroll
  for (int n = 0; n < 16 * NR; ++n) { kg[n % NR][n / NR] = ld128(c.rs, gv[n % NR], gs + (n / NR) * KVSLOT); SB(); }
  {
    f32x4 sum[NR];
    bool redo = false;
#pragma unroll
    for (int i = 0; i < 16; ++i) {
      unsigned m = 0;
#pragma unroll
      for (int r = 0; r < NR; ++r) { m = mx4(m, kg[r][i]); sum[r] = i ? sum[r] + __builtin_bit_cast(f32x4, kg[r][i]) : __builtin_bit_cast(f32x4, kg[r][i]); OPQV(sum[r]); }
      if (__builtin_expect(__any(m == POISON), 0)) {
        redo = true;
        int gs2 = gs; OPQ(gs2);
#pragma unroll
        for (int r = 0; r < NR; ++r) kg[r][i] = ld128(c.rs, gv[r], gs2 + i * KVSLOT);
      }
    }
    if (__builtin_expect(redo, 0)) {
      NOUNROLL for (unsigned spins = 0; spins < SPIN_CAP; ++spins) {
        RCOUNT(1);
        bool any = false;
        int gs2 = gs; OPQ(gs2);
        __builtin_amdgcn_s_waitcnt(0x0F70);
#pragma unroll
        for (int i = 0; i < 16; ++i)
#pragma unroll
          for (int r = 0; r < NR; ++r) sum[r] = i ? sum[r] + __builtin_bit_cast(f32x4, kg[r][i]) : __builtin_bit_cast(f32x4, kg[r][i]);
        float t = sum[0][1] + sum[0][3];
        if (NR == 2) t += sum[NR - 1][1] + sum[NR - 1][3];
        if (!__any(t != t)) break;
#pragma unroll
        for (int i = 0; i < 16; ++i) {
          unsigned m = 0;
#pragma unroll
          for (int r = 0; r < NR; ++r) m = mx4(m, kg[r][i]);
          if (__any(m == POISON)) {
            any = true;
#pragma unroll
            for (int r = 0; r < NR; ++r) kg[r][i] = ld128(c.rs, gv[r], gs2 + i * KVSLOT);
          }
        }
        if (!any) break;
      }
      __builtin_amdgcn_s_waitcnt(0x0F70);
    }
#pragma unroll
    for (int r = 0; r < NR; ++r) *(LAS v2u*)(L + kvl[r]) = (v2u){pk2(sum[r][0], sum[r][1]), pk2(sum[r][2], sum[r][3])};
  }
  SB();
  bar();
#pragma unroll
  for (int k = WE; k < 25; ++k) stw(sd + k * 1024, wst[k]);
  SB();
  bar();
#pragma unroll
  for (int n = Q4; n < Q5; ++n) bg(n);
  __builtin_amdgcn_s_sleep(HS);
#pragma unroll
  for (int n = Q5; n < 63; ++n) { bg(n); __builtin_amdgcn_s_sleep(SL3); }
  {
    LAS char* ud = wa + 102400 + w * 12800;
    LAS char* ul = hh ? L + L_DUMP : ud + 12 * 1024;
#pragma unroll
    for (int k = 0; k < 13; ++k) stw(k < 12 ? ud + k * 1024 : ul, up[k]);
  }
  RAWBAR();
#pragma unroll
  for (int k = 0; k < 25; ++k) stw(sd + k * 1024, nxt[k]);
  SB();
}

DI void set_ctx(Ctx& c, const Params& p, int b, char* smem, int wv = -1) {
  c.L = (LAS char*)smem;
  c.rs = __builtin_amdgcn_make_buffer_rsrc(p.ws, 0, WS_USED, 0x00020000);
  const int xcd = b & 7, r = b >> 3;
  c.pi = 4 * (xcd >> 1) + (r & 3); c.pj = 8 * (xcd & 1) + (r >> 2); c.th = c.pj >> 3; c.cs = c.pj & 7;
  c.lane = threadIdx.x & 63; c.w = wv >= 0 ? wv : (__builtin_amdgcn_readfirstlane(threadIdx.x >> 6) & 3);
  c.xo = R_X + (4 * c.pi + 2 * c.th + (c.w >> 1)) * 40960 + 5 * c.cs * 1024 + (c.w & 1) * 256;
  c.oo = R_O + (c.pi * 8 + 2 * c.w) * 20480 + 5 * c.pj * 256;
  c.ko = R_KV + (c.pj * 16 + c.pi) * KVSLOT;
  OPQ(c.xo); OPQ(c.oo); OPQ(c.ko);
}
constexpr int CODE_KB = 40;
__global__ void __launch_bounds__(512, 2) mega(Params p) {
  extern __shared__ __attribute__((aligned(16))) char smem[];
  unsigned long long pc; asm volatile("s_getpc_b64 %0" : "=s"(pc));
  Ctx c;
  set_ctx(c, p, blockIdx.x, smem);
  const int w8 = __builtin_amdgcn_readfirstlane(threadIdx.x >> 6);
  LAS char* L = c.L;
  const int lane = c.lane, w = c.w, t16 = lane & 15, g = lane >> 4;
  int l16 = lane * 16;
  { const int i_ = threadIdx.x; ZST(i_); }
  if (w8 < 4) {
    STAMP(0); CSTAMP(120);
    unsigned xp[10];
    const int xov = (g >> 1) * 512 + t16 * 16 + (g & 1) * 8;
#pragma unroll
    for (int ct = 0; ct < 5; ++ct) { const v2u v = __builtin_amdgcn_raw_buffer_load_b64(c.rs, xov, c.xo + ct * 1024, 0); xp[2 * ct] = v.x; xp[2 * ct + 1] = v.y; }
    for (int it = 0; it < 2; ++it) {
      layer_c<false>(c, p, 2 * it, xp);
      layer_c<true>(c, p, 2 * it + 1, xp);
    }
    STAMP(100); CSTAMP(121);
  } else {
    {
      const unsigned pf = *(const GAS unsigned*)((pc & ~127ull) + 128u * min(((blockIdx.x >> 3) & 7u) * 64u + ((threadIdx.x & 255u) >> 2), (unsigned)(CODE_KB * 8 - 1)));
      SB();
      const int wt0 = OFF_WT + c.pj * WT_SZ;
      v4u im[38];
#pragma unroll
      for (int k = 0; k < 38; ++k) im[k] = k < 25 ? ldi(c.rs, l16, wt0 + w * 25600, k * 1024) : ldi(c.rs, l16, wt0 + w * 12800, 102400 + (k - 25) * 1024);
      const int i = (w & 1) * 64 + lane;
      unsigned cs[20];
#pragma unroll
      for (int k = 0; k < 20; ++k) cs[k] = __builtin_amdgcn_raw_buffer_load_b32(c.rs, i * 4 + (k & 7) * 512, OFF_CS + ((w >> 1) * 16 + c.pj) * 10240 + (k >> 3) * 4096, 0);
      if (w == 0 && lane < 24) *(LAS unsigned*)(L + L_ZERO + 4 * lane) = 0u;
      if (w == 1 && lane < 2) *(LAS unsigned*)(L + L_GO + 4 * lane) = 0u;
      SB();
      LAS char* ud = L + l16 + 102400 + w * 12800; LAS char* S = L + l16 + w * 25600;
#pragma unroll
      for (int k = 0; k < 38; ++k) *(LAS v4u*)(k < 25 ? S + k * 1024 : (k < 37 ? ud + (k - 25) * 1024 : (lane < 32 ? ud + 12 * 1024 : L + L_DUMP))) = im[k];
      asm volatile("" :: "v"(pf));
      float s = 0.f;
#pragma unroll
      for (int k = 0; k < 20; ++k) s += u2f(cs[k]);
      const float hi = rbf(s), mid = rbf(s - hi), lo = rbf(s - hi - mid);
      *(LAS v2u*)(L + L_CS + (w >> 1) * 1024 + i * 8) = (v2u){pk2(hi, mid), pk2(lo, 0.f)};
    }
    STAMP(108);
    for (int it = 0; it < 2; ++it) {
      layer_h<false>(c, 2 * it);
      layer_h<true>(c, 2 * it + 1);
    }
  }
  DUMP();
}

__global__ void __launch_bounds__(64) prep(Params p) {
  __shared__ __attribute__((aligned(16))) char smem[12288 + 256];
  LAS char* S = (LAS char*)smem;
  const int it = blockIdx.x, lane = threadIdx.x, t16 = lane & 15, g = lane >> 4, q4 = t16 >> 2, p4 = lane & 3, l16 = lane * 16;
  const rsrc_t rs = __builtin_amdgcn_make_buffer_rsrc(p.ws, 0, WS_USED, 0x00020000);
  if (it < 640) {
    const int type = it >= 320, rem = it - 320 * type, j = rem & 15, kb = rem >> 4;
    const rsrc_t rw = __builtin_amdgcn_make_buffer_rsrc((void*)(type ? p.wqkv[1] : p.wqkv[0]), 0, 640 * 3840, 0x00020000);
    const int vq = t16 < 15 ? g * 3840 + t16 * 16 + (t16 >= 5 ? 1200 : 0) + (t16 >= 10 ? 1200 : 0) : OOR;
    v4u cw[8]; v2u cp[8];
#pragma unroll
    for (int k = 0; k < 8; ++k) cw[k] = __builtin_amdgcn_raw_buffer_load_b128(rw, vq, j * 80 + (32 * kb + 4 * k) * 3840, 0);
    if (type) {
      const rsrc_t rp = __builtin_amdgcn_make_buffer_rsrc((void*)p.wphi, 0, 32 * 800, 0x00020000);
      const int d = lane >> 3, q8 = lane & 7;
#pragma unroll
      for (int k = 0; k < 8; ++k) cp[k] = __builtin_amdgcn_raw_buffer_load_b64(rp, (d + 8 * (k & 3) < 20 && q8 < 5) ? d * 40 + q8 * 8 : OOR, (2 * j + (k >> 2)) * 800 + (k & 3) * 320, 0);
    }
    SB();
#pragma unroll
    for (int k = 0; k < 8; ++k) *(LAS v4u*)(S + l16 + k * 1024) = cw[k];
    if (type) {
#pragma unroll
      for (int k = 0; k < 8; ++k) *(LAS v2u*)(S + 8192 + lane * 8 + k * 512) = cp[k];
      LAS char* pa = S + 8192 + 64 * (8 * g + q4) + 8 * p4;
      LAS char* pr = S + t16 * 256;
      LAS char* pb = pr + (g < 3 ? g * 16 : 0); LAS char* pb2 = pb + (g == 2 ? 0 : 8);
      f32x4 o0[8], o1[8];
#pragma unroll
      for (int u = 0; u < 8; ++u) {
        const bf16x8 f0 = tr2(pa + (u & 1) * 2048, 0, 256), f1 = tr2(pa + (u & 1) * 2048, 32, 288);
        const v2u lo = *(LAS v2u*)(pb + (u & 3) * 40 + (u >> 2) * 4096), hi = *(LAS v2u*)(pb2 + (u & 3) * 40 + (u >> 2) * 4096);
        const bf16x8 bb = asb((v4u){lo.x, lo.y, hi.x, hi.y});
        o0[u] = mfma16(f0, bb, (f32x4){}); o1[u] = mfma16(f1, bb, (f32x4){});
      }
      SB();
#pragma unroll
      for (int u = 0; u < 8; ++u) {
        *(LAS v2u*)(pr + 8 * g + (u & 3) * 40 + (u >> 2) * 4096) = (v2u){pk2(o0[u][0], o0[u][1]), pk2(o0[u][2], o0[u][3])};
        *(LAS v2u*)(g == 0 ? pr + 32 + (u & 3) * 40 + (u >> 2) * 4096 : S + 12288) = (v2u){pk2(o1[u][0], o1[u][1]), pk2(o1[u][2], o1[u][3])};
      }
    }
    SB();
    const int head = p4 & 1, sq = 2 * (g & 1) + (p4 >> 1), r32 = lane & 31;
    LAS char* pt = S + (8 * (g >> 1) + q4) * 256;
    f32x4 s4 = {0.f, 0.f, 0.f, 0.f};
#pragma unroll
    for (int T = 0; T < 4; ++T) {
      const int tpos = T == 2 ? (sq == 0 ? 80 : (sq == 1 ? 160 : 0)) + 32 + 8 * head : (T == 3 ? 0 : 80 + 80 * T) + 48 * head + 8 * sq;
      const int tv = T == 2 ? (r32 < 24 ? (g >> 1) * 384 + r32 * 16 : OOR) : l16;
#pragma unroll
      for (int h = 0; h < 2; ++h) {
        const v4u f = __builtin_bit_cast(v4u, tr2(pt + tpos + h * 4096, 0, 1024));
        stp128(rs, tv, OFF_WT + (16 * type + j) * WT_SZ + (T == 3 ? L_T3 : T * 40960) + (2 * kb + h) * (T == 2 ? 768 : 1024), f);
        s4[T] = sum2(f.x, sum2(f.y, sum2(f.z, sum2(f.w, s4[T]))));
      }
      s4[T] = xsum32(s4[T]);
    }
    stp128(rs, g < 2 ? l16 : OOR, OFF_CS + (type * 16 + j) * 10240 + kb * 512, __builtin_bit_cast(v4u, s4));
  } else if (it < 896) {
    const int q = it - 640, tc = q & 15, i = q >> 4;
    const rsrc_t ro = __builtin_amdgcn_make_buffer_rsrc((void*)((tc >> 3) ? p.wo[1] : p.wo[0]), 0, 640 * 1280, 0x00020000);
    const int vo = t16 < 10 ? g * 1280 + t16 * 16 : OOR;
    v4u cw[10];
#pragma unroll
    for (int k = 0; k < 10; ++k) cw[k] = __builtin_amdgcn_raw_buffer_load_b128(ro, vo, (tc & 7) * 160 + (40 * i + 4 * k) * 1280, 0);
    SB();
#pragma unroll
    for (int k = 0; k < 10; ++k) *(LAS v4u*)(S + l16 + k * 1024) = cw[k];
#pragma unroll
    for (int k = 0; k < 7; ++k) {
      const int gi = 4 * k + g, gc = min(gi, 24), rg = (gc * 13) >> 6, ct = gc - rg * 5, RG = 5 * i + rg;
      const bf16x8 f = tr2(S + (8 * rg + q4) * 256 + 32 * ct + 8 * p4, 0, 1024);
      stp128(rs, gi < 25 ? (((RG >> 2) * 5 + ct) * 64 + (RG & 3) * 16 + t16) * 16 : OOR, OFF_WO + tc * WO_SZ, __builtin_bit_cast(v4u, f));
    }
  } else {
    const int q = it - 896;
    Ctx c;
    set_ctx(c, p, q & 255, smem, q >> 8);
    unsigned xp[10];
    const unsigned short* xr = p.x + (size_t)(128 * c.pi + 64 * c.th + 16 * c.w + t16) * 640 + 80 * c.cs + 4 * g;
#pragma unroll
    for (int ct = 0; ct < 5; ++ct) { const v2u v = *(const GAS v2u*)(xr + 16 * ct); xp[2 * ct] = v.x; xp[2 * ct + 1] = v.y; }
    const v4u ps = {POISON, POISON, POISON, POISON};
    int bq = c.oo + 1024, dq = c.oo + 1024 + 20480 - 256, hq = OFF_HB + q * 32 - 512; OPQ(bq); OPQ(dq); OPQ(hq);
    stp128(rs, l16, c.oo, ps); stp128(rs, l16, c.oo + 20480, ps);
    stp128(rs, lane < 34 ? l16 + (lane < 16 ? bq : (lane < 32 ? dq : hq)) : OOR, 0, ps);
    stp128(rs, l16, c.ko + 512 + c.w * 1024, ps);
    publish_x(c, 0, (g >> 1) * 512 + t16 * 16 + (g & 1) * 8, xp);
    publish_stats(c, 0, lane, xp);
  }
}

extern "C" void kernel_launch(void* const* d_in, const int* in_sizes, int n_in, void* d_out, int out_size, void* d_ws, size_t ws_size, hipStream_t stream) {
  Params p;
  p.x = (const unsigned short*)d_in[0];
  p.wqkv[0] = (const unsigned short*)d_in[1]; p.wo[0] = (const unsigned short*)d_in[2];
  p.wqkv[1] = (const unsigned short*)d_in[4]; p.wphi = (const unsigned short*)d_in[5]; p.wo[1] = (const unsigned short*)d_in[6];
  p.ws = (unsigned char*)d_ws; p.out = (unsigned short*)d_out;
  (void)hipFuncSetAttribute((const void*)mega, hipFuncAttributeMaxDynamicSharedMemorySize, LDS_BYTES);
  prep<<<1920, 64, 0, stream>>>(p);
  mega<<<256, 512, LDS_BYTES, stream>>>(p);
}
```

```cpp
#include <hip/hip_runtime.h>
#include <hip/hip_bf16.h>
#include <stdint.h>

typedef unsigned v4u __attribute__((ext_vector_type(4)));
typedef unsigned v2u __attribute__((ext_vector_type(2)));
typedef short bf16x8 __attribute__((ext_vector_type(8)));
typedef short s16x4 __attribute__((ext_vector_type(4)));
typedef float f32x2 __attribute__((ext_vector_type(2)));
typedef float f32x4 __attribute__((ext_vector_type(4)));
typedef float f32x16 __attribute__((ext_vector_type(16)));
typedef __bf16 bf16x2v __attribute__((ext_vector_type(2)));
#define LAS __attribute__((address_space(3)))
#define GAS __attribute__((address_space(1)))
#define DI __device__ __forceinline__
#define AI __attribute__((always_inline))
typedef __amdgpu_buffer_rsrc_t rsrc_t;

constexpr int SC1 = 16;
constexpr unsigned POISON = 0xFFFFFFFFu;
constexpr unsigned SPIN_CAP = 1u << 16;
constexpr int OOR = 0x7FFF0000;

constexpr int XB_SZ = 2048 * 640 * 2;
constexpr int KVSLOT = 8192;
constexpr int R_X = 0, R_O = XB_SZ, R_KV = 2 * XB_SZ;
constexpr int LSZ = 2 * XB_SZ + 256 * KVSLOT;
constexpr int WT_SZ = 153600, WO_SZ = 102400;
constexpr int OFF_WT = 4 * LSZ;
constexpr int OFF_CS = OFF_WT + 2 * 16 * WT_SZ;
constexpr int OFF_WO = OFF_CS + 32 * 10240;
constexpr int OFF_HB = OFF_WO + 2 * 8 * WO_SZ;
constexpr int OFF_CTRL = OFF_HB + 32768;
constexpr int F_X = 4096, F_R = F_X + 16384, F_H = F_R + 16384, F_A = F_H + 16384;
constexpr int OFF_DBG = OFF_CTRL + F_A + 16384;
constexpr int WS_USED = OFF_DBG + (1 << 20);

constexpr int L_Q = 102400, L_MISC = 153600;
constexpr int L_KV = L_MISC;
constexpr int L_ZERO = L_KV + 4032;
constexpr int L_DUMP = L_ZERO + 96;
constexpr int L_CS = L_DUMP + 16;
constexpr int L_ST = L_CS + 2048;
constexpr int L_GO = L_ST + 1024;
constexpr int LDS_BYTES = 163840;

#define OPQ(x) do { x = __builtin_amdgcn_readfirstlane(x); asm volatile("" : "+s"(x)); } while (0)
#define OPQV(x) asm volatile("" : "+v"(x))
#define PINA(x) OPQV(x)
#define SB() __builtin_amdgcn_sched_barrier(0)
#define DRAIN() asm volatile("s_waitcnt vmcnt(0)" ::: "memory")
#define VMWAIT(n) asm volatile("s_waitcnt vmcnt(%0)" :: "i"(n) : "memory")
#define NOUNROLL _Pragma("nounroll")
DI v4u ld128(rsrc_t rs, int voff, int soff) { return __builtin_amdgcn_raw_buffer_load_b128(rs, voff, soff, SC1); }
DI v2u ld64(rsrc_t rs, int voff, int soff) { return __builtin_amdgcn_raw_buffer_load_b64(rs, voff, soff, SC1); }
DI v4u ldi(rsrc_t rs, int& voff, int soff, int coff) { OPQV(voff); return ld128(rs, voff + (coff & 4095), soff + (coff & ~4095)); }
DI unsigned np(unsigned u) { return u < 0xFFFFFFFEu ? u : 0xFFFFFFFEu; }
DI void st128r(rsrc_t rs, int voff, int soff, v4u v) { __builtin_amdgcn_raw_buffer_store_b128(v, rs, voff, soff, SC1); asm volatile("s_nop 1" :: "v"(v)); }
DI void st128(rsrc_t rs, int voff, int soff, v4u v) { st128r(rs, voff, soff, (v4u){v.x, np(v.y), v.z, np(v.w)}); }
DI void st64(rsrc_t rs, int voff, int soff, v2u v) { v2u q = {v.x, np(v.y)}; __builtin_amdgcn_raw_buffer_store_b64(q, rs, voff, soff, SC1); }
DI void stp128(rsrc_t rs, int voff, int soff, v4u v) { __builtin_amdgcn_raw_buffer_store_b128(v, rs, voff, soff, 0); asm volatile("s_nop 1" :: "v"(v)); }
DI void poison128(rsrc_t rs, int voff, int soff) { st128r(rs, voff, soff, (v4u){POISON, POISON, POISON, POISON}); }
DI unsigned pk2(float a, float b) { f32x2 v = {a, b}; return __builtin_bit_cast(unsigned, __builtin_convertvector(v, bf16x2v)); }
DI float u2f(unsigned u) { return __builtin_bit_cast(float, u); }
DI unsigned f2u(float f) { return __builtin_bit_cast(unsigned, f); }
DI float rbf(float a) { return u2f(pk2(a, a) & 0xFFFF0000u); }
DI float blo(unsigned u) { return u2f(u << 16); }
DI float bhi(unsigned u) { return u2f(u & 0xFFFF0000u); }
DI float sum2(unsigned u, float acc) { return __builtin_amdgcn_fdot2_f32_bf16(__builtin_bit_cast(bf16x2v, u), __builtin_bit_cast(bf16x2v, 0x3F803F80u), acc, false); }
DI unsigned umax3(unsigned a, unsigned b, unsigned c) { return max(max(a, b), c); }
DI unsigned mx4(unsigned m, v4u v) { return umax3(m, v.y, v.w); }
DI bf16x8 tr2(LAS char* p, int off1, int off2) {
  s16x4 a = __builtin_amdgcn_ds_read_tr16_b64_v4i16((LAS s16x4*)(p + off1));
  s16x4 b = __builtin_amdgcn_ds_read_tr16_b64_v4i16((LAS s16x4*)(p + off2));
  return __builtin_shufflevector(a, b, 0, 1, 2, 3, 4, 5, 6, 7);
}
DI bf16x8 rd(LAS char* p, int off) { return *(LAS bf16x8*)(p + off); }
DI f32x16 mfma32(bf16x8 a, bf16x8 b, f32x16 c) { return __builtin_amdgcn_mfma_f32_32x32x16_bf16(a, b, c, 0, 0, 0); }
DI f32x4 mfma16(bf16x8 a, bf16x8 b, f32x4 c) { return __builtin_amdgcn_mfma_f32_16x16x32_bf16(a, b, c, 0, 0, 0); }
DI bf16x8 asb(v4u v) { return __builtin_bit_cast(bf16x8, v); }
DI float xsum32(float v) { unsigned u = f2u(v); auto r = __builtin_amdgcn_permlane32_swap(u, u, false, false); return u2f(r[0]) + u2f(r[1]); }
DI float xsum16(float v) { unsigned u = f2u(v); auto r = __builtin_amdgcn_permlane16_swap(u, u, false, false); return u2f(r[0]) + u2f(r[1]); }
DI int lane_id() { int l; asm volatile("v_mbcnt_lo_u32_b32 %0, -1, 0\n\tv_mbcnt_hi_u32_b32 %0, -1, %0" : "=v"(l)); return l; }
DI unsigned vzero() { unsigned z; asm volatile("v_mov_b32 %0, 0" : "=v"(z)); return z; }
DI unsigned vone() { unsigned z; asm volatile("v_mov_b32 %0, 0x3f80" : "=v"(z)); return z; }
DI void bar() { asm volatile("s_waitcnt lgkmcnt(0)" ::: "memory"); __builtin_amdgcn_s_barrier(); asm volatile("" ::: "memory"); }
#define MF16(acc, a, b) asm volatile("v_mfma_f32_16x16x32_bf16 %0, %1, %2, %0" : "+v"(acc) : "v"(a), "v"(b))
#define MFIN() asm volatile("s_nop 7\n\ts_nop 1" ::: "memory")
#define MF32(acc, a, b) do { acc = mfma32(a, b, acc); PINA(acc); SB(); } while (0)
DI void stw(LAS char* p, v4u& v) { *(LAS v4u*)p = v; }
#define RAWBAR() do { SB(); __builtin_amdgcn_s_barrier(); SB(); } while (0)
template <int I> struct IC { static constexpr int v = I; };
template <int B, int E, class F> DI void sfor(F&& f) { if constexpr (B < E) { f(IC<B>{}); sfor<B + 1, E>(f); } }

#define STAMP(k) do {} while (0)
#define CL 2
#define CSTAMP(k) do {} while (0)
#define DUMP()
#define ZST(i)
#define RCOUNT(k)
#define LSTAMP(k) do { STAMP(1 + lyr * 24 + (k)); if ((k) >= 2 && (k) < 8 && lyr == CL) CSTAMP(112 + (k)); } while (0)

struct Params {
  const unsigned short* x; const unsigned short* wqkv[2]; const unsigned short* wo[2]; const unsigned short* wphi;
  unsigned char* ws; unsigned short* out;
};
struct Ctx { rsrc_t rs; LAS char* L; int pi, pj, th, cs, w, lane; int xo, oo, ko; v2u ep; };

template <int N> DI unsigned chk128(const v4u* x) { unsigned m = 0;
#pragma unroll
  for (int k = 0; k < N; ++k) m = mx4(m, x[k]);
  return m; }
template <int N, int G0, int G1>
DI void fixup(const Ctx& c, v4u* x, unsigned mk, int voff, int soff, int stride, int kind) {
  if (__builtin_expect(__any(mk == POISON), 0)) {
    NOUNROLL for (unsigned spins = 0; spins < SPIN_CAP; ++spins) {
      OPQ(soff);
      bool bad[G1 - G0];
#pragma unroll
      for (int q = G0; q < G1; ++q) bad[q - G0] = __any(chk128<N>(x + N * q) == POISON);
      SB();
#pragma unroll
      for (int q = G0; q < G1; ++q) {
        if (bad[q - G0]) {
#pragma unroll
          for (int k = N * q; k < N * q + N; ++k) x[k] = ld128(c.rs, voff, soff + k * stride);
        }
      }
      if (!bad[0]) break;
      RCOUNT(kind);
    }
    __builtin_amdgcn_s_waitcnt(0x0F70);
  }
}

template <bool ISB>
DI void phi20(const float* v, float rs, unsigned* out) {
  const float rl = rs * 1.44269504088896f;
  if constexpr (!ISB) {
    float r[20];
#pragma unroll
    for (int i = 0; i < 20; ++i) {
      const float y = v[i] * rl;
      r[i] = fmaf(__builtin_amdgcn_fmed3f(y, 1.44269504e-6f, __builtin_inff()), 0.693147180559945f, __builtin_amdgcn_fmed3f(__builtin_amdgcn_exp2f(y), 0.f, 1.f));
    }
#pragma unroll
    for (int i = 0; i < 10; ++i) out[i] = pk2(r[2 * i], r[2 * i + 1]);
  } else {
    float mx = __builtin_amdgcn_fmed3f(v[0], v[1], __builtin_inff()), mn = __builtin_amdgcn_fmed3f(v[0], v[1], -__builtin_inff());
#pragma unroll
    for (int i = 2; i < 20; i += 2) { mx = fmaxf(fmaxf(mx, v[i]), v[i + 1]); mn = fminf(fminf(mn, v[i]), v[i + 1]); }
    const float cp = -mx * rl, cn = mn * rl;
    const f32x2 rl2 = {rl, rl}, cp2 = {cp, cp}, cn2 = {cn, cn};
    f32x2 ep[10], en[10], sp, sn;
#pragma unroll
    for (int i = 0; i < 10; ++i) {
      const f32x2 a = {v[2 * i], v[2 * i + 1]};
      const f32x2 yp = __builtin_elementwise_fma(a, rl2, cp2), yn = __builtin_elementwise_fma(a, -rl2, cn2);
      ep[i] = (f32x2){__builtin_amdgcn_exp2f(yp[0]), __builtin_amdgcn_exp2f(yp[1])};
      en[i] = (f32x2){__builtin_amdgcn_exp2f(yn[0]), __builtin_amdgcn_exp2f(yn[1])};
      sp = i ? sp + ep[i] : ep[i]; sn = i ? sn + en[i] : en[i];
    }
    const float ip = __builtin_amdgcn_rcpf(sp[0] + sp[1]), in = __builtin_amdgcn_rcpf(sn[0] + sn[1]);
#pragma unroll
    for (int i = 0; i < 10; ++i) { const f32x2 a = ep[i] * ip, b = en[i] * in; out[i] = pk2(a[0], a[1]); out[10 + i] = pk2(b[0], b[1]); }
  }
}
DI void swap16(float a, float b, float& lo, float& hi) { auto r = __builtin_amdgcn_permlane16_swap(f2u(a), f2u(b), false, false); lo = u2f(r[0]); hi = u2f(r[1]); }

DI void poison_all(const Ctx& c, int lb, int n, int l16, int lane) {
  const int odd = c.cs & 1, s0 = (5 * c.cs + odd) >> 1;
  int xf = c.xo + (s0 * 2 + (c.w & 1)) * 1024, a = c.xo + (odd ? s0 - 1 : s0 + 2) * 2048 + odd * 512, b = c.oo + 1024 - 512, d = c.oo + 1024 + 20480 - 768;
  OPQ(xf); OPQ(a); OPQ(b); OPQ(d);
  poison128(c.rs, l16, lb + xf); poison128(c.rs, l16 + 1024, lb + xf);
  poison128(c.rs, l16, lb + c.oo); poison128(c.rs, l16, lb + c.oo + 20480);
  poison128(c.rs, l16 + (lane < 32 ? a : (lane < 48 ? b : d)), lb);
  poison128(c.rs, l16, lb + c.ko + c.w * 1024);
  if (n > 6) poison128(c.rs, l16, lb + c.ko + c.w * 1024 + 4096);
}
DI int x_piece(const Ctx& c, int ct) { const int ch = 10 * c.cs + 2 * ct; return c.xo + (ch >> 2) * 2048 + (ch & 3) * 256; }
DI void uswap16(unsigned a, unsigned b, unsigned& lo, unsigned& hi) { auto r = __builtin_amdgcn_permlane16_swap(a, b, false, false); lo = r[0]; hi = r[1]; }
DI void x_chunks(const unsigned* xp, v4u* ch) {
#pragma unroll
  for (int k = 0; k < 3; ++k) {
    unsigned a0, a1, b0, b1;
    uswap16(xp[4 * k], xp[k < 2 ? 4 * k + 2 : 8], a0, b0); uswap16(xp[4 * k + 1], xp[k < 2 ? 4 * k + 3 : 9], a1, b1);
    ch[k] = (v4u){a0, a1, b0, b1};
  }
}
DI int x_voff(const Ctx& c, int lane) { int d = (c.cs & 1) ? 1536 : 512; OPQ(d); return ((lane >> 4) & 1) * d + (lane >> 5) * 256 + (lane & 15) * 16; }
DI void publish_x(const Ctx& c, int lb, int xv, int xv4, const v4u* ch) {
#pragma unroll
  for (int k = 0; k < 3; ++k) st128(c.rs, k < 2 ? xv : xv4, lb + x_piece(c, 2 * k), ch[k]);
}
DI void publish_stats(const Ctx& c, int lb, int lane, const unsigned* xp) {
  float s = 0.f;
#pragma unroll
  for (int i = 0; i < 10; ++i) s = sum2(xp[i], s);
  s = xsum32(xsum16(s));
  const float m = s * (1.f / 80.f);
  f32x2 q2 = {0.f, 0.f};
#pragma unroll
  for (int i = 0; i < 10; ++i) { const f32x2 d = (f32x2){blo(xp[i]), bhi(xp[i])} - m; q2 = __builtin_elementwise_fma(d, d, q2); }
  const float q = xsum32(xsum16(q2[0] + q2[1]));
  st64(c.rs, lane < 16 ? lane * 8 : OOR, lb + c.ko + c.w * 128, (v2u){f2u(s), f2u(q)});
}
#define HINT 0
#define PD3 0
#define PS 1
#define OPF 10
#define PD1 4
#define PD2 4
DI v2u next_epoch(v2u e) { unsigned x = __builtin_amdgcn_readfirstlane(e.x) + 1u, y = __builtin_amdgcn_readfirstlane(e.y) + (x == 0u); return (v2u){x, y}; }
DI void wait_flags(const Ctx& c, int voff, int soff, int kind) {
  NOUNROLL for (unsigned spins = 0; spins < SPIN_CAP; ++spins) {
    const v2u f = ld64(c.rs, voff, OFF_CTRL + soff);
    if (__all(f.x == c.ep.x && f.y == c.ep.y)) break;
    RCOUNT(kind);
    __builtin_amdgcn_s_sleep(2);
  }
  asm volatile("" ::: "memory");
}
DI void set_flag(const Ctx& c, int voff) { __builtin_amdgcn_raw_buffer_store_b64(c.ep, c.rs, voff, OFF_CTRL, SC1); }
#define PW0 8
#define PW1 8
#define XRA 10
#define XRN 12
#define XE 4
#define PAN 8
#define XPN 16
#define RQ 3
#define B4N 69
template <bool ISB>
DI void layer_c(const Ctx& c, const Params& p, const int lyr) {
  unsigned xp[10]; int xrv, xr4; v4u xc[3];
  int lane = lane_id();
  const int w = c.w, t32 = lane & 31, hh = lane >> 5, t16 = lane & 15, g = lane >> 4;
  constexpr int NQ = ISB ? 10 : 5, NKS = ISB ? 5 : 3, TRS = ISB ? 160 : 96;
  constexpr int type = ISB ? 1 : 0;
  LAS char* L = c.L;
  int l16 = lane * 16;
  LAS char* wa = L + l16;
  const int lb = __builtin_amdgcn_readfirstlane(lyr * LSZ);
  v4u xf[40];
  const int xs = lb + R_X + (c.pi * 4 + w) * 40960;
  const int q4 = (lane & 15) >> 2, p4 = lane & 3;
  LAS char* wh = wa + 51200;
  constexpr int VO = ISB ? 80 : 48;
  LAS char* dst = L + hh * 20480 + (32 * w + t32) * TRS;
  LAS char* ta = L + (w & 1) * 20480 + (4 * g + q4) * TRS + 8 * p4;
  LAS char* tb = ta + VO + 32 * (w >> 1) - (((w >> 1) && p4 >= 2) ? 16 : 0);
  int kvv = 16 * (w >> 1) + t16 < 21 ? g * 336 + (16 * (w >> 1) + t16) * 16 : OOR;
  int kvw = g < NQ % 4 ? kvv : OOR;
  LAS char* csa = L + (g ? L_ZERO : L_CS + type * 1024 + t16 * 64);
  LAS char* wq = L + L_ST + w * 256 + t16 * 8;
  LAS char* rsa = L + L_ST + w * 256 + t32 * 8 + 4;
  OPQV(wh); OPQV(dst); OPQV(ta); OPQV(tb); OPQV(kvv); OPQV(kvw); OPQV(csa); OPQV(wq); OPQV(rsa);
  SB();
  if (ISB || lyr) { VMWAIT(1); __builtin_amdgcn_s_sleep(PD1); }
  LSTAMP(1);
  constexpr int XP = XPN, XR = ISB ? XRN : XRA;
#pragma unroll
  for (int k = 0; k < XP; ++k) { xf[k] = ldi(c.rs, l16, xs, k * 1024); SB(); }
  RAWBAR();
  LSTAMP(2);
  f32x4 a[5][2] = {};
  constexpr int PA = PAN;
  bf16x8 A[PA];
#pragma unroll
  for (int t = 0; t < 5; ++t) { OPQV(a[t][0]); OPQV(a[t][1]); }
  SB();
#pragma unroll
  for (int n = 0; n < PA; ++n) A[n] = rd(wa, n * 1024);
  SB();
  float rs = 0.f; v4u cs[4], mlo[2], mhi[2]; unsigned mm[2];
  LAS char* wq2;
  unsigned mk = chk128<4>(xf);
  sfor<0, 100>([&](auto N) AI {
    constexpr int n = decltype(N)::v, s = n / 5, t = n % 5, gq = n / 10, u = n % 10;
    if constexpr (u == 0) { fixup<4, gq, (XP / 4 + gq < 10 ? XP / 4 + gq : 10)>(c, xf, mk, l16, xs, 1024, 0); SB(); }
    if constexpr ((n & 1) == 0) { OPQV(A[(n + 1) % PA]); SB(); }
    MF16(a[t][0], A[n % PA], asb(xf[2 * s])); SB();
    if constexpr ((u == 0 || u == 2 || u == 4 || u == 8) && XP + 4 * gq + (u == 8 ? 3 : u / 2) < 40) { constexpr int k = XP + 4 * gq + (u == 8 ? 3 : u / 2); xf[k] = ldi(c.rs, l16, xs, k * 1024); }
    if constexpr (gq < 9 && (u == 5 || u == 7)) { mk = mx4(u == 5 ? 0u : mk, xf[4 * gq + u]); OPQV(mk); }
    if constexpr (n == 70) { mm[0] = *(LAS unsigned*)(wq); mm[1] = *(LAS unsigned*)(wq + 128); }
    if constexpr (n == 71) { rs = *(LAS float*)(rsa); }
    if constexpr (n == 72) { cs[0] = *(LAS v4u*)(csa); }
    if constexpr (n == 73) { cs[1] = *(LAS v4u*)(csa + 16); }
    if constexpr (n == 74) { cs[2] = *(LAS v4u*)(csa + 32); }
    if constexpr (n == 84 || n == 85) { mlo[n - 84].z = vzero(); mlo[n - 84].w = vzero(); }
    if constexpr (n == 86 || n == 87) { mhi[n - 86].x = vzero(); mhi[n - 86].y = vzero(); }
    if constexpr (n == 88) { OPQV(mlo[0]); OPQV(mlo[1]); OPQV(mhi[0]); OPQV(mhi[1]); }
    if constexpr (n == 80 || n == 81) { mlo[n - 80].x = mm[n - 80]; mlo[n - 80].y = mm[n - 80]; }
    if constexpr (n == 82 || n == 83) { mhi[n - 82].z = mm[n - 82]; mhi[n - 82].w = mm[n - 82]; }
    SB();
    MF16(a[t][1], A[n % PA], asb(xf[2 * s + 1])); SB();
    if constexpr (n + PA < 100) A[n % PA] = (n + PA < 50) ? rd(wa, (n + PA) * 1024) : rd(wh, (n + PA - 50) * 1024);
    if constexpr (gq < 9 && (u == 6 || u == 8)) { mk = mx4(mk, xf[4 * gq + u - 2]); OPQV(mk); }
    SB();
    if constexpr (n >= 90 && n < 95) {
      constexpr int q = (n + 2) % 5;
      MF16(a[q][0], asb(cs[q >> 1]), asb((q & 1) ? mhi[0] : mlo[0])); SB();
      MF16(a[q][1], asb(cs[q >> 1]), asb((q & 1) ? mhi[1] : mlo[1])); SB();
    }
    if constexpr (n == B4N) RAWBAR();
  });
  MFIN();
#pragma unroll
  for (int t = 0; t < 5; ++t) { OPQV(a[t][0]); OPQV(a[t][1]); }
  SB();
  LSTAMP(3);
  {
    float kx[20], vx[20];
#pragma unroll
    for (int i = 0; i < 20; ++i) swap16(a[i >> 2][0][i & 3], a[i >> 2][1][i & 3], kx[i], vx[i]);
    unsigned v[10], o[ISB ? 20 : 10];
#pragma unroll
    for (int i = 0; i < 10; ++i) v[i] = pk2(vx[2 * i] * rs, vx[2 * i + 1] * rs);
    *(LAS v4u*)(dst + VO) = (v4u){v[0], v[1], v[2], v[3]}; *(LAS v4u*)(dst + VO + 16) = (v4u){v[4], v[5], v[6], v[7]};
    *(LAS v4u*)(dst + VO + 32) = (v4u){v[8], v[9], vone(), vzero()};
    phi20<ISB>(kx, rs, o);
#pragma unroll
    for (int i = 0; i < (ISB ? 5 : 2); ++i) *(LAS v4u*)(dst + 16 * i) = (v4u){o[4 * i], o[4 * i + 1], o[4 * i + 2], o[4 * i + 3]};
    if (!ISB) *(LAS v2u*)(dst + 32) = (v2u){o[8], o[9]};
  }
  SB();
  bar();
  LSTAMP(4);
  {
    constexpr int NF = ISB ? 3 : 2;
    bf16x8 fa[4][NF], fb[4];
    f32x4 kv[NF] = {};
#pragma unroll
    for (int ks = 0; ks < 6; ++ks) {
      if (ks < 4) {
        fb[ks] = tr2(tb, 32 * ks * TRS, (32 * ks + 16) * TRS);
#pragma unroll
        for (int ft = 0; ft < NF; ++ft) fa[ks][ft] = tr2(ta, 32 * ks * TRS + 32 * ft, (32 * ks + 16) * TRS + 32 * ft);
        SB();
      }
      if (ks >= 1 && ks - 1 < XE && ks - 1 < XR) { xf[ks - 1] = ldi(c.rs, l16, xs, (ks - 1) * 1024); SB(); }
      if (ks >= 2) {
#pragma unroll
        for (int ft = NF - 1; ft >= 0; --ft) { kv[ft] = mfma16(fa[ks - 2][ft], fb[ks - 2], kv[ft]); OPQV(kv[ft]); }
        SB();
      }
    }
    const int kvs = lb + c.ko + 512 + (w & 1) * NQ * 336;
    wq = wa + L_Q; wq2 = L + L_Q + 2048 + g * 128 + (t16 >> 3) * 64 + (t16 & 3) * 16;
    xrv = x_voff(c, lane); xr4 = (g & 1) ? OOR : xrv;
    OPQV(wq); OPQV(wq2); OPQV(xrv); OPQV(xr4);
    SB();
#pragma unroll
    for (int ft = NF - 1; ft >= 0; --ft) st128(c.rs, (ft < NF - 1 ? kvv : kvw) + ft * 1344, kvs, __builtin_bit_cast(v4u, kv[ft]));
  }
  RAWBAR();
  LSTAMP(5);
  int k0, k1, ov, xov;
  unsigned pq[NKS * 4];
  {
    f32x4 q[3][2] = {};
#pragma unroll
    for (int t = 0; t < 3; ++t) { OPQV(q[t][0]); OPQV(q[t][1]); }
    SB();
    auto rdq = [&](int n) AI { const int s = (n / 3 + XR / 2) % 20, t = n % 3; return t < 2 ? rd(wq, s * 2560 + t * 1024) : rd(wq2, s * 2560); };
#pragma unroll
    for (int n = 0; n < PA; ++n) A[n] = rdq(n);
    cs[2] = *(LAS v4u*)(csa + 32); cs[3] = *(LAS v4u*)(csa + 48);
    OPQV(mm[0]); OPQV(mm[1]);
#pragma unroll
    for (int h = 0; h < 2; ++h) { mlo[h] = (v4u){mm[h], mm[h], vzero(), vzero()}; mhi[h] = (v4u){vzero(), vzero(), mm[h], mm[h]}; OPQV(mlo[h]); OPQV(mhi[h]); }
    SB();
    sfor<0, 60>([&](auto N) AI {
      constexpr int n = decltype(N)::v, s = (n / 3 + XR / 2) % 20, t = n % 3;
      if constexpr ((n & 1) == 0) { OPQV(A[(n + 1) % PA]); SB(); }
      MF16(q[t][0], A[n % PA], asb(xf[2 * s])); SB();
      if constexpr (n % RQ == 0 && XE + n / RQ < XR) xf[XE + n / RQ] = ldi(c.rs, l16, xs, (XE + n / RQ) * 1024);
      if constexpr (n == 41 || n == 44 || n == 47) { constexpr int k = (n - 41) / 3; xc[k] = ld128(c.rs, k < 2 ? xrv : xr4, lb + x_piece(c, 2 * k)); }
      SB();
      MF16(q[t][1], A[n % PA], asb(xf[2 * s + 1])); SB();
      if constexpr (n + PA < 60) A[n % PA] = rdq(n + PA);
      SB();
      if constexpr (n >= 6 && n < 9) {
        constexpr int z = (n + 1) % 3;
        MF16(q[z][0], asb(cs[(5 + z) >> 1]), asb((z & 1) ? mlo[0] : mhi[0]));
        MF16(q[z][1], asb(cs[(5 + z) >> 1]), asb((z & 1) ? mlo[1] : mhi[1]));
        SB();
      }
    });
    MFIN();
#pragma unroll
    for (int t = 0; t < 3; ++t) { OPQV(q[t][0]); OPQV(q[t][1]); }
    SB();
    LSTAMP(6);
    {
      const int e0 = 4 * (t32 >> 3) + (t32 & 3), e1 = min(16 + e0, 20);
      const bool own = ((t32 >> 2) & 1) == hh;
      k0 = own ? L_KV + hh * 2016 + e0 * 96 : L_ZERO; k1 = own ? L_KV + hh * 2016 + e1 * 96 : L_ZERO;
      xov = (t32 >> 4) * 20480 + t16 * 16; ov = xov + 768 * hh; xov = hh ? OOR : xov;
      OPQV(k0); OPQV(k1); OPQV(ov); OPQV(xov);
    }
    float qx[24];
#pragma unroll
    for (int i = 0; i < 12; ++i) swap16(q[i >> 2][0][i & 3], q[i >> 2][1][i & 3], qx[i], qx[12 + i]);
    phi20<ISB>(qx, rs, pq);
#pragma unroll
    for (int i = 0; i < (ISB ? 20 : 10); ++i) OPQV(pq[i]);
    if (!ISB) { pq[10] = vzero(); pq[11] = vzero(); }
  }
  f32x4 acc[5];
  {
    unsigned d0, d1;
#pragma unroll
    for (int k = 0; k < 2; ++k) { uswap16(xc[k].x, xc[k].z, xp[4 * k], xp[4 * k + 2]); uswap16(xc[k].y, xc[k].w, xp[4 * k + 1], xp[4 * k + 3]); }
    uswap16(xc[2].x, xc[2].z, xp[8], d0); uswap16(xc[2].y, xc[2].w, xp[9], d1);
  }
#pragma unroll
  for (int ct = 0; ct < 5; ++ct) { acc[ct] = (f32x4){blo(xp[2 * ct]), bhi(xp[2 * ct]), blo(xp[2 * ct + 1]), bhi(xp[2 * ct + 1])}; OPQV(acc[ct]); }
  LSTAMP(7);
  RAWBAR();
  LSTAMP(8);
  {
    bf16x8 f0[NKS], f1[NKS];
#pragma unroll
    for (int ks = 0; ks < NKS; ++ks) f1[ks] = rd(L + k1, 16 * ks);
#pragma unroll
    for (int ks = 0; ks < NKS; ++ks) f0[ks] = rd(L + k0, 16 * ks);
    SB();
    f32x16 n0 = {}, n1 = {};
#pragma unroll
    for (int ks = 0; ks < NKS; ++ks) MF32(n1, f1[ks], asb((v4u){pq[4 * ks], pq[4 * ks + 1], pq[4 * ks + 2], pq[4 * ks + 3]}));
    float id = 0.f;
    const int os = lb + c.oo;
#pragma unroll
    for (int ks = 0; ks < NKS; ++ks) {
      MF32(n0, f0[ks], asb((v4u){pq[4 * ks], pq[4 * ks + 1], pq[4 * ks + 2], pq[4 * ks + 3]}));
      if (ks == NKS - 2) { OPQV(n1); id = __builtin_amdgcn_rcpf(n1[4] + 1e-6f); OPQV(id); }
      if (ks == NKS - 1) {
        const unsigned m0 = pk2(n1[0] * id, n1[1] * id), m1 = pk2(n1[2] * id, n1[3] * id);
        auto r0 = __builtin_amdgcn_permlane32_swap(m0, m0, false, false); auto r1 = __builtin_amdgcn_permlane32_swap(m1, m1, false, false);
        st128(c.rs, xov + 512, os, (v4u){r0[0], r1[0], r0[1], r1[1]});
      }
      SB();
    }
    unsigned d[8];
#pragma unroll
    for (int i = 0; i < 8; ++i) d[i] = pk2(n0[2 * i] * id, n0[2 * i + 1] * id);
    st128(c.rs, ov, os, (v4u){d[0], d[1], d[2], d[3]});
    st128(c.rs, ov + 256, os, (v4u){d[4], d[5], d[6], d[7]});
  }
  RAWBAR();
  LSTAMP(9);
  constexpr int WPD = 32;
  bf16x8 wf[WPD];
#pragma unroll
  for (int k = 0; k < WPD; ++k) wf[k] = rd(wa, k * 1024);
  SB();
  VMWAIT(0);
  __builtin_amdgcn_s_sleep(PD2);
  LSTAMP(10);
  v4u of[20];
  const int oos = lb + R_O + (c.pi * 8 + 4 * c.th + w) * 20480;
#pragma unroll
  for (int s = 0; s < OPF; ++s) { of[s] = ldi(c.rs, l16, oos, s * 1024); SB(); }
  mk = chk128<5>(of);
  sfor<0, 100>([&](auto N) AI {
    constexpr int n = decltype(N)::v, s = n / 5, ct = n % 5, gq = n / 25, u = n % 25;
    if constexpr (u == 0) { fixup<5, gq, ((OPF + 6 * gq) / 5 < 4 ? (OPF + 6 * gq) / 5 : 4)>(c, of, mk, l16, oos, 1024, 2); SB(); }
    if constexpr (n >= WPD && (n & 3) == 0) { OPQV(wf[(n + 3) % WPD]); SB(); }
    MF16(acc[ct], wf[n % WPD], asb(of[s])); SB();
    if constexpr (n % 4 != 3) { constexpr int m = WPD + 3 * (n / 4) + n % 4; if constexpr (m < 100) wf[m % WPD] = (m < 50) ? rd(wa, m * 1024) : rd(wh, (m - 50) * 1024); }
    if constexpr (n % 4 == 3 && OPF + n / 4 < 20) of[OPF + n / 4] = ldi(c.rs, l16, oos, (OPF + n / 4) * 1024);
    if constexpr (gq < 3 && u >= 14 && u < 24 && (u & 1) == 0) { constexpr int q = (u - 14) / 2; mk = mx4(q == 0 ? 0u : mk, of[5 * gq + 5 + q]); OPQV(mk); }
    SB();
  });
  MFIN();
#pragma unroll
  for (int t = 0; t < 5; ++t) OPQV(acc[t]);
  SB();
  LSTAMP(11);
#pragma unroll
  for (int ct = 0; ct < 5; ++ct) { xp[2 * ct] = pk2(acc[ct][0], acc[ct][1]); xp[2 * ct + 1] = pk2(acc[ct][2], acc[ct][3]); }
  x_chunks(xp, xc);
  if (lyr < 3) {
    publish_x(c, lb + LSZ, xrv, xr4, xc);
    RAWBAR();
    publish_stats(c, lb + LSZ, lane, xp);
  } else {
    unsigned short* o = p.out + (size_t)(128 * c.pi + 64 * c.th + 16 * w + t16) * 640 + 80 * c.cs + 16 * (g & 1) + 8 * (g >> 1);
    *(GAS v4u*)o = xc[0]; *(GAS v4u*)(o + 32) = xc[1];
    if (!(g & 1)) *(GAS v4u*)(o + 64) = xc[2];
  }
  SB();
  LSTAMP(0);
}

#define KM 55
#define Q1 10
#define SL1 1
#define Q2 20
#define SL2 0
#define Q3 20
#define Q4 25
#define WE 14
#define Q5 40
#define HS 10
#define SL3 0
template <bool ISB>
DI void layer_h(const Ctx& c, const int lyr) {
  int lane = lane_id();
  const int w = c.w, t32 = lane & 31, hh = lane >> 5;
  constexpr int NQ = ISB ? 10 : 5, NP = 2 * NQ * 21;
  constexpr int type = ISB ? 1 : 0;
  LAS char* L = c.L;
  int l16 = lane * 16;
  LAS char* wa = L + l16;
  LAS char* sd = wa + w * 25600;
  const int lb = __builtin_amdgcn_readfirstlane(lyr * LSZ);
  int wos = OFF_WO + (type * 8 + c.cs) * WO_SZ + w * 25600;
  int wt0 = OFF_WT + ((1 - type) * 16 + c.pj) * WT_SZ;
  if (lyr == 3) wt0 = OOR - 0x1000000;
  OPQ(wos); OPQ(wt0);
  bar();
  if (!ISB && lyr == 0) {
    *(volatile LAS unsigned*)(L + L_GO + 8) = ~c.ep.x;
    DRAIN();
    int f0 = F_H + ((c.pj * 4 + w) * 16 + c.pi) * 16, f1 = F_R + ((c.pi * 4 + w) * 16 + c.pj) * 16; OPQ(f0); OPQ(f1);
    set_flag(c, lane == 0 ? f0 : (lane == 1 ? f1 : OOR));
    if (w >= 2) wait_flags(c, min(lane, 19) * 32 + 16, F_A + c.pj * 1024, 6);
  }
  {
    v2u stv[4];
    const int sv = hh * (4 * 16 * KVSLOT) + t32 * 8; int ss = lb + R_KV + ((w >> 1) * 128 + c.pi) * KVSLOT + (w & 1) * 256;
    NOUNROLL for (unsigned spins = 0; spins < SPIN_CAP; ++spins) {
#pragma unroll
      for (int q = 0; q < 4; ++q) stv[q] = ld64(c.rs, sv, ss + q * 16 * KVSLOT);
      if (!__any(max(umax3(stv[0].y, stv[1].y, stv[2].y), stv[3].y) == POISON)) break;
      RCOUNT(3);
      __builtin_amdgcn_s_sleep(4);
    }
    const float mean = xsum32((u2f(stv[0].x) + u2f(stv[1].x)) + (u2f(stv[2].x) + u2f(stv[3].x))) * (1.f / 640.f);
    float m2 = 0.f;
#pragma unroll
    for (int q = 0; q < 4; ++q) { const float d = u2f(stv[q].x) * (1.f / 80.f) - mean; m2 += fmaf(80.f * d, d, u2f(stv[q].y)); }
    const float var = rbf(xsum32(m2) * (1.f / 640.f));
    const float rs = __builtin_amdgcn_rcpf(rbf(__builtin_amdgcn_sqrtf(rbf(var + 1e-5f))));
    *(LAS v2u*)(L + L_ST + w * 256 + t32 * 8) = (v2u){pk2(-mean, -mean), f2u(rs)};
  }
  auto colsum = [&](int t) AI {
    const int i = (w & 1) * 64 + lane;
    unsigned cs[20];
#pragma unroll
    for (int k = 0; k < 20; ++k) cs[k] = __builtin_amdgcn_raw_buffer_load_b32(c.rs, i * 4 + (k & 7) * 512, OFF_CS + (t * 16 + c.pj) * 10240 + (k >> 3) * 4096, SC1);
    float s = 0.f;
#pragma unroll
    for (int k = 0; k < 20; ++k) s += u2f(cs[k]);
    const float hi = rbf(s), mid = rbf(s - hi), lo = rbf(s - hi - mid);
    *(LAS v2u*)(L + L_CS + t * 1024 + i * 8) = (v2u){pk2(hi, mid), pk2(lo, 0.f)};
  };
  if (lyr == (int)ISB && w >= 2) colsum(type);
  if (!ISB && lyr == 0) {
    v4u u0[13];
#pragma unroll
    for (int k = 0; k < 13; ++k) u0[k] = ldi(c.rs, l16, OFF_WT + c.pj * WT_SZ + w * 12800, 102400 + k * 1024);
    LAS char* ud = wa + 102400 + w * 12800;
#pragma unroll
    for (int k = 0; k < 13; ++k) stw(k < 12 ? ud + k * 1024 : (hh ? L + L_DUMP : ud + 12 * 1024), u0[k]);
  }
  constexpr int NR = ISB ? 2 : 1, PW = NP / 4 + (ISB ? 0 : 1);
  int gv[NR], kvl[NR];
#pragma unroll
  for (int r = 0; r < NR; ++r) {
    const int pn = lane + 64 * r, pc = w * PW + pn; const bool ok = pn < PW && pc < NP;
    const int hf = (pc * 3121) >> 16, zi = w * 11 + lane - 53;
    gv[r] = ok ? pc * 16 : OOR;
    kvl[r] = ok ? L_KV + (pc - hf * 21) * 96 + hf * 8 + (hf >= NQ ? 2016 - NQ * 8 : 0) : ((!ISB && lane >= 53 && zi < 42) ? L_KV + zi * 96 + 40 : L_DUMP);
    OPQV(gv[r]); OPQV(kvl[r]);
  }
  v4u wst[25], up[13], nxt[25];
  auto bg = [&](int n) AI {
    if (n < 25) wst[n] = ldi(c.rs, l16, wos, n * 1024);
    else if (n < 38) up[n - 25] = ldi(c.rs, l16, wt0 + w * 12800, 102400 + (n - 25) * 1024);
    else if (n < 63) nxt[n - 38] = ldi(c.rs, l16, wt0 + w * 25600, (n - 38) * 1024);
    SB();
  };
  if (ISB || lyr) {
#pragma unroll
    for (int n = 0; n < Q1; ++n) { bg(n); __builtin_amdgcn_s_sleep(SL1); }
  }
  bar();
  const unsigned t4 = (unsigned)__builtin_amdgcn_s_memrealtime();
  const int hb = OFF_HB + lyr * 8192;
  if (w == 0) __builtin_amdgcn_raw_buffer_store_b64((v2u){t4, c.ep.x}, c.rs, lane == 0 ? 0 : OOR, hb + (c.pi * 16 + c.pj) * 32, SC1);
  if (!ISB && lyr == 0) {
    wait_flags(c, l16, F_H + (c.pj & 7) * 1024, 7);
    if (w < 2) wait_flags(c, l16, w ? F_H + (c.pj | 8) * 1024 : F_R + c.pi * 1024, 7);
#pragma unroll
    for (int n = 0; n < Q1; ++n) bg(n);
  }
  poison_all(c, lyr < 3 ? lb + LSZ : OOR, ISB ? 6 : 7, l16, lane);
#pragma unroll
  for (int n = Q1; n < Q2; ++n) { bg(n); __builtin_amdgcn_s_sleep(SL2); }
  VMWAIT(Q2 - Q1);
  RAWBAR();
#pragma unroll
  for (int n = Q2; n < Q3; ++n) bg(n);
  v2u hv[4];
  const int hvo = w == 3 ? lane * 32 : OOR;
#pragma unroll
  for (int q = 0; q < 4; ++q) hv[q] = ld64(c.rs, hvo, hb + q * 2048);
  RAWBAR();
  const unsigned t6 = (unsigned)__builtin_amdgcn_s_memrealtime();
  v4u kg[NR][16];
  const int gs = lb + R_KV + c.pj * 16 * KVSLOT + 512;
#pragma unroll
  for (int k = 0; k < WE; ++k) { if (Q3 + k < Q4) bg(Q3 + k); stw(sd + k * 1024, wst[k]); SB(); }
  {
    volatile LAS unsigned* go = (volatile LAS unsigned*)(L + L_GO) + (lyr & 1);
    int lag = 0;
    if (w == 3) {
      NOUNROLL for (unsigned spins = 0; spins < 64; ++spins) {
        bool ok = true; lag = 0;
#pragma unroll
        for (int q = 0; q < 4; ++q) { ok &= hv[q].y == c.ep.x; lag = max(lag, (int)(hv[q].x - t4)); }
        if (__all(ok)) break;
        RCOUNT(5);
#pragma unroll
        for (int q = 0; q < 4; ++q) if (__any(hv[q].y != c.ep.x)) hv[q] = ld64(c.rs, hvo, hb + q * 2048);
        lag = 0;
      }
#pragma unroll
      for (int d = 32; d >= 1; d >>= 1) lag = max(lag, __shfl_xor(lag, d));
      lag = min(lag, 400);
      *go = ((unsigned)(lyr + 1) << 16) | (unsigned)lag;
    } else {
      unsigned v = 0;
      NOUNROLL for (unsigned spins = 0; spins < 4096; ++spins) { v = *go; if ((v >> 16) == (unsigned)(lyr + 1)) break; __builtin_amdgcn_s_sleep(1); }
      lag = v & 0x1FF;
    }
    lag = __builtin_amdgcn_readfirstlane(lag) + KM;
    NOUNROLL while ((int)((unsigned)__builtin_amdgcn_s_memrealtime() - t6) < lag) __builtin_amdgcn_s_sleep(1);
  }
#pragma unroll
  for (int n = 0; n < 16 * NR; ++n) { kg[n % NR][n / NR] = ld128(c.rs, gv[n % NR], gs + (n / NR) * KVSLOT); SB(); }
  {
    f32x4 sum[NR];
    bool redo = false;
#pragma unroll
    for (int i = 0; i < 16; ++i) {
      unsigned m = 0;
#pragma unroll
      for (int r = 0; r < NR; ++r) { m = mx4(m, kg[r][i]); sum[r] = i ? sum[r] + __builtin_bit_cast(f32x4, kg[r][i]) : __builtin_bit_cast(f32x4, kg[r][i]); OPQV(sum[r]); }
      if (__builtin_expect(__any(m == POISON), 0)) {
        redo = true;
        int gs2 = gs; OPQ(gs2);
#pragma unroll
        for (int r = 0; r < NR; ++r) kg[r][i] = ld128(c.rs, gv[r], gs2 + i * KVSLOT);
      }
    }
    if (__builtin_expect(redo, 0)) {
      NOUNROLL for (unsigned spins = 0; spins < SPIN_CAP; ++spins) {
        RCOUNT(1);
        bool any = false;
        int gs2 = gs; OPQ(gs2);
        __builtin_amdgcn_s_waitcnt(0x0F70);
#pragma unroll
        for (int i = 0; i < 16; ++i)
#pragma unroll
          for (int r = 0; r < NR; ++r) sum[r] = i ? sum[r] + __builtin_bit_cast(f32x4, kg[r][i]) : __builtin_bit_cast(f32x4, kg[r][i]);
        float t = sum[0][1] + sum[0][3];
        if (NR == 2) t += sum[NR - 1][1] + sum[NR - 1][3];
        if (!__any(t != t)) break;
#pragma unroll
        for (int i = 0; i < 16; ++i) {
          unsigned m = 0;
#pragma unroll
          for (int r = 0; r < NR; ++r) m = mx4(m, kg[r][i]);
          if (__any(m == POISON)) {
            any = true;
#pragma unroll
            for (int r = 0; r < NR; ++r) kg[r][i] = ld128(c.rs, gv[r], gs2 + i * KVSLOT);
          }
        }
        if (!any) break;
      }
      __builtin_amdgcn_s_waitcnt(0x0F70);
    }
#pragma unroll
    for (int r = 0; r < NR; ++r) *(LAS v2u*)(L + kvl[r]) = (v2u){pk2(sum[r][0], sum[r][1]), pk2(sum[r][2], sum[r][3])};
  }
  SB();
  bar();
#pragma unroll
  for (int k = WE; k < 25; ++k) stw(sd + k * 1024, wst[k]);
  SB();
  bar();
  if (lyr == 3) return;
#pragma unroll
  for (int n = Q4; n < Q5; ++n) bg(n);
  {
    LAS char* ud = wa + 102400 + w * 12800;
    LAS char* ul = hh ? L + L_DUMP : ud + 12 * 1024;
#pragma unroll
    for (int k = 0; k < 13; ++k) stw(k < 12 ? ud + k * 1024 : ul, up[k]);
  }
  SB();
  __builtin_amdgcn_s_sleep(HS);
#pragma unroll
  for (int n = Q5; n < 63; ++n) { bg(n); __builtin_amdgcn_s_sleep(SL3); }
  RAWBAR();
#pragma unroll
  for (int k = 0; k < 25; ++k) stw(sd + k * 1024, nxt[k]);
  SB();
}

DI void set_ctx(Ctx& c, const Params& p, int b, char* smem, int wv = -1) {
  c.L = (LAS char*)smem;
  c.rs = __builtin_amdgcn_make_buffer_rsrc(p.ws, 0, WS_USED, 0x00020000);
  const int xcd = b & 7, r = b >> 3;
  c.pi = 4 * (xcd >> 1) + (r & 3); c.pj = 8 * (xcd & 1) + (r >> 2); c.th = c.pj >> 3; c.cs = c.pj & 7;
  c.lane = threadIdx.x & 63; c.w = wv >= 0 ? wv : (__builtin_amdgcn_readfirstlane(threadIdx.x >> 6) & 3);
  c.xo = R_X + (4 * c.pi + 2 * c.th + (c.w >> 1)) * 40960 + (c.w & 1) * 1024;
  c.oo = R_O + (c.pi * 8 + 2 * c.w) * 20480 + 5 * c.pj * 256;
  c.ko = R_KV + (c.pj * 16 + c.pi) * KVSLOT;
  OPQ(c.xo); OPQ(c.oo); OPQ(c.ko);
}
constexpr int CODE_KB = 48;
__global__ void __launch_bounds__(512, 2) mega(Params p) {
  extern __shared__ __attribute__((aligned(16))) char smem[];
  unsigned pf;
  {
    unsigned long long pc; asm volatile("s_getpc_b64 %0" : "=s"(pc));
    const unsigned ln = min(((blockIdx.x >> 3) & 3u) * 128u + (threadIdx.x >> 2), (unsigned)(CODE_KB * 8 - 1));
    asm volatile("global_load_dword %0, %1, %2" : "=v"(pf) : "v"(ln * 128u), "s"(pc & ~127ull) : "memory");
  }
  Ctx c;
  set_ctx(c, p, blockIdx.x, smem);
  const int w8 = __builtin_amdgcn_readfirstlane(threadIdx.x >> 6);
  LAS char* L = c.L;
  const int lane = c.lane, w = c.w, t16 = lane & 15, g = lane >> 4, q4 = t16 >> 2, p4 = lane & 3;
  int l16 = lane * 16;
  { const int i_ = threadIdx.x; ZST(i_); }
  v2u ep;
  if (w8 < 4) {
    STAMP(0); CSTAMP(120);
    const rsrc_t rx = __builtin_amdgcn_make_buffer_rsrc((void*)p.x, 0, 2048 * 1280, 0x00020000);
    const int xa = t16 * 1280 + 32 * (g & 1) + 16 * (g >> 1);
    v4u xc[3];
#pragma unroll
    for (int k = 0; k < 3; ++k) xc[k] = __builtin_amdgcn_raw_buffer_load_b128(rx, (k == 2 && (g & 1)) ? OOR : xa + 64 * k, (128 * c.pi + 64 * c.th + 16 * w) * 1280 + 160 * c.cs, 0);
    ep = ld64(c.rs, 0, OFF_CTRL);
    const int xv = x_voff(c, lane);
    SB();
    publish_x(c, 0, xv, (g & 1) ? OOR : xv, xc);
    unsigned xp[10], d0, d1;
#pragma unroll
    for (int k = 0; k < 2; ++k) { xp[4 * k] = xc[k].x; xp[4 * k + 1] = xc[k].y; xp[4 * k + 2] = xc[k].z; xp[4 * k + 3] = xc[k].w; }
    uswap16(xc[2].x, xc[2].z, xp[8], d0); uswap16(xc[2].y, xc[2].w, xp[9], d1);
    publish_stats(c, 0, lane, xp);
    c.ep = next_epoch(ep);
    int f0 = F_X + (((c.pi * 2 + c.th) * 4 + w) * 8 + c.cs) * 16; OPQ(f0);
    STAMP(101);
    DRAIN();
    set_flag(c, lane == 0 ? f0 : OOR);
    STAMP(102);
    __builtin_amdgcn_s_sleep(8);
    wait_flags(c, t16 * 16, F_X + (c.pi * 4 + w) * 256, 4);
    STAMP(103);
  } else {
    LAS char* S = L + L_Q + w * 12800;
    const int kind = w == 2 ? 2 : (w == 3 ? (c.pi < 8 ? c.pi >> 2 : 3) : w), kb = w == 3 ? 16 + (c.pi & 3) : c.pi;
    const rsrc_t rw = __builtin_amdgcn_make_buffer_rsrc((void*)(kind == 2 ? (c.th ? p.wo[1] : p.wo[0]) : (kind ? p.wqkv[1] : p.wqkv[0])), 0, kind == 3 ? 0 : (kind == 2 ? 640 * 1280 : 640 * 3840), 0x00020000);
    const int vq = kind == 2 ? (t16 < 10 ? g * 1280 + t16 * 16 : OOR) : (t16 < 15 ? g * 3840 + t16 * 16 + (t16 >= 5 ? 1200 : 0) + (t16 >= 10 ? 1200 : 0) : OOR);
    const int so = kind == 2 ? c.cs * 160 + 40 * kb * 1280 : c.pj * 80 + 32 * kb * 3840, st = kind == 2 ? 4 * 1280 : 4 * 3840;
    v2u cp[8];
    {
      v4u cw[10];
#pragma unroll
      for (int k = 0; k < 10; ++k) cw[k] = __builtin_amdgcn_raw_buffer_load_b128(rw, vq, (k < 8 || kind == 2) ? so + k * st : OOR, 0);
      ep = ld64(c.rs, 0, OFF_CTRL);
      if (kind == 1) {
        const rsrc_t rp = __builtin_amdgcn_make_buffer_rsrc((void*)p.wphi, 0, 32 * 800, 0x00020000);
        const int d = lane >> 3, q8 = lane & 7;
#pragma unroll
        for (int k = 0; k < 8; ++k) cp[k] = __builtin_amdgcn_raw_buffer_load_b64(rp, (d + 8 * (k & 3) < 20 && q8 < 5) ? d * 40 + q8 * 8 : OOR, (2 * c.pj + (k >> 2)) * 800 + (k & 3) * 320, 0);
      }
      SB();
#pragma unroll
      for (int k = 0; k < 10; ++k) *(LAS v4u*)(S + l16 + k * 1024) = cw[k];
    }
    c.ep = next_epoch(ep);
    if (w == 0 && lane < 24) *(LAS unsigned*)(L + L_ZERO + 4 * lane) = 0u;
    if (kind == 1) {
#pragma unroll
        for (int k = 0; k < 8; ++k) *(LAS v2u*)(S + 8192 + lane * 8 + k * 512) = cp[k];
        LAS char* pa = S + 8192 + 64 * (8 * g + q4) + 8 * p4;
        LAS char* pr = S + t16 * 256;
        LAS char* pb = pr + (g < 3 ? g * 16 : 0); LAS char* pb2 = pb + (g == 2 ? 0 : 8);
        f32x4 o0[8], o1[8];
#pragma unroll
        for (int u = 0; u < 8; ++u) {
          const bf16x8 f0 = tr2(pa + (u & 1) * 2048, 0, 256), f1 = tr2(pa + (u & 1) * 2048, 32, 288);
          const v2u lo = *(LAS v2u*)(pb + (u & 3) * 40 + (u >> 2) * 4096), hi = *(LAS v2u*)(pb2 + (u & 3) * 40 + (u >> 2) * 4096);
          const bf16x8 bb = asb((v4u){lo.x, lo.y, hi.x, hi.y});
          o0[u] = mfma16(f0, bb, (f32x4){}); o1[u] = mfma16(f1, bb, (f32x4){});
        }
        SB();
#pragma unroll
        for (int u = 0; u < 8; ++u) {
          *(LAS v2u*)(pr + 8 * g + (u & 3) * 40 + (u >> 2) * 4096) = (v2u){pk2(o0[u][0], o0[u][1]), pk2(o0[u][2], o0[u][3])};
          *(LAS v2u*)(g == 0 ? pr + 32 + (u & 3) * 40 + (u >> 2) * 4096 : S + 12288) = (v2u){pk2(o1[u][0], o1[u][1]), pk2(o1[u][2], o1[u][3])};
        }
        SB();
    }
    v4u f[8];
    if (kind != 2) {
      LAS char* pt = S + (8 * g + q4) * 256 + (p4 >> 1) * 40;
      const int hd = p4 >> 1;
#pragma unroll
      for (int T = 0; T < 8; ++T) {
        const int sq = T < 5 ? T : ((p4 & 1) ? (T < 7 ? T - 2 : 2) : T - 5);
        const int qd = sq + hd >= 5 ? sq + hd - 5 : sq + hd;
        f[T] = __builtin_bit_cast(v4u, tr2(pt + (T < 5 ? 80 + 80 * (p4 & 1) : 0) + 8 * qd, 0, 1024));
      }
    } else {
#pragma unroll
      for (int k = 0; k < 8; ++k) {
        const int gc = min(4 * k + g, 24), rg = (gc * 13) >> 6, ct = gc - rg * 5;
        f[k] = __builtin_bit_cast(v4u, tr2(S + (8 * rg + q4) * 256 + 32 * ct + 8 * p4, 0, 1024));
      }
    }
    SB();
    v4u im[25];
    volatile LAS unsigned* go = (volatile LAS unsigned*)(L + L_GO + 8);
    NOUNROLL for (int pass = 0; pass < 2; ++pass) {
      if (pass == 1) {
        if (w == 0) {
          __builtin_amdgcn_s_sleep(PW1);
          wait_flags(c, min(lane, 19) * 32, F_A + c.pj * 1024, 6);
          go[0] = c.ep.x; go[1] = ~c.ep.y;
        } else {
          NOUNROLL for (unsigned spins = 0; spins < (SPIN_CAP << 4); ++spins) { if (go[0] == c.ep.x && go[1] == ~c.ep.y) break; __builtin_amdgcn_s_sleep(1); }
          asm volatile("" ::: "memory");
        }
        STAMP(106);
#pragma unroll
        for (int k = 0; k < 25; ++k) im[k] = ldi(c.rs, l16, OFF_WT + c.pj * WT_SZ + w * 25600, k * 1024);
        SB();
      }
      if (pass == kind) {
        const int tv7 = (t16 & 4) ? OOR : g * 128 + (t16 >> 3) * 64 + (t16 & 3) * 16, ib = OFF_WT + (16 * kind + c.pj) * WT_SZ;
#pragma unroll
        for (int T = 0; T < 8; ++T) st128r(c.rs, T < 7 ? l16 : tv7, ib + (T < 5 ? (kb * 5 + T) * 1024 : L_Q + kb * 2560 + (T - 5) * 1024), f[T]);
        if (kind == 0) {
          STAMP(104);
          DRAIN();
          int f0 = F_A + (c.pj * 32 + kb) * 32; OPQ(f0);
          set_flag(c, lane == 0 ? f0 : OOR);
          STAMP(105);
        }
        float sm[8];
#pragma unroll
        for (int T = 0; T < 8; ++T) sm[T] = xsum32(xsum16(sum2(f[T].x, sum2(f[T].y, sum2(f[T].z, sum2(f[T].w, 0.f))))));
        st128r(c.rs, g < 2 ? t16 * 32 + g * 16 : OOR, OFF_CS + (kind * 16 + c.pj) * 10240 + kb * 512, g ? (v4u){f2u(sm[4]), f2u(sm[5]), f2u(sm[6]), f2u(sm[7])} : (v4u){f2u(sm[0]), f2u(sm[1]), f2u(sm[2]), f2u(sm[3])});
        if (kind == 0) {
          DRAIN();
          int f0 = F_A + (c.pj * 32 + kb) * 32 + 16; OPQ(f0);
          set_flag(c, lane == 0 ? f0 : OOR);
        }
      }
    }
    if (kind == 2) {
#pragma unroll
      for (int k = 0; k < 7; ++k) {
        const int gi = 4 * k + g, rg = (gi * 13) >> 6, ct = gi - rg * 5, RG = 5 * c.pi + rg;
        st128r(c.rs, gi < 25 ? (((RG >> 2) * 5 + ct) * 64 + (RG & 3) * 16 + t16) * 16 : OOR, OFF_WO + c.pj * WO_SZ, f[k]);
      }
    }
    int bq = c.oo + 1024, dq = c.oo + 1024 + 20480 - 256; OPQ(bq); OPQ(dq);
    poison128(c.rs, l16, c.oo); poison128(c.rs, l16, c.oo + 20480);
    poison128(c.rs, lane < 32 ? l16 + (lane < 16 ? bq : dq) : OOR, 0);
    poison128(c.rs, l16, c.ko + 512 + w * 1024);
    SB();
#pragma unroll
    for (int k = 0; k < 25; ++k) *(LAS v4u*)(L + w * 25600 + l16 + k * 1024) = im[k];
    if (w == 1 && lane < 2) *(LAS unsigned*)(L + L_GO + 4 * lane) = 0u;
    STAMP(108);
  }
  asm volatile("" :: "v"(pf));
  int role = w8; OPQ(role);
  if (role < 4) {
    for (int it = 0; it < 2; ++it) {
      layer_c<false>(c, p, 2 * it);
      layer_c<true>(c, p, 2 * it + 1);
    }
    if (blockIdx.x == 0 && w == 0) set_flag(c, lane_id() == 0 ? 0 : OOR);
    STAMP(100); CSTAMP(121);
  } else {
    for (int it = 0; it < 2; ++it) {
      layer_h<false>(c, 2 * it);
      layer_h<true>(c, 2 * it + 1);
    }
  }
  DUMP();
}

extern "C" void kernel_launch(void* const* d_in, const int* in_sizes, int n_in, void* d_out, int out_size, void* d_ws, size_t ws_size, hipStream_t stream) {
  Params p;
  p.x = (const unsigned short*)d_in[0];
  p.wqkv[0] = (const unsigned short*)d_in[1]; p.wo[0] = (const unsigned short*)d_in[2];
  p.wqkv[1] = (const unsigned short*)d_in[4]; p.wphi = (const unsigned short*)d_in[5]; p.wo[1] = (const unsigned short*)d_in[6];
  p.ws = (unsigned char*)d_ws; p.out = (unsigned short*)d_out;
  (void)hipFuncSetAttribute((const void*)mega, hipFuncAttributeMaxDynamicSharedMemorySize, LDS_BYTES);
  mega<<<256, 512, LDS_BYTES, stream>>>(p);
}
```

```cpp
#include <hip/hip_runtime.h>
#include <hip/hip_bf16.h>
#include <stdint.h>

typedef unsigned v4u __attribute__((ext_vector_type(4)));
typedef unsigned v2u __attribute__((ext_vector_type(2)));
typedef short bf16x8 __attribute__((ext_vector_type(8)));
typedef short s16x4 __attribute__((ext_vector_type(4)));
typedef float f32x2 __attribute__((ext_vector_type(2)));
typedef float f32x4 __attribute__((ext_vector_type(4)));
typedef float f32x16 __attribute__((ext_vector_type(16)));
typedef __bf16 bf16x2v __attribute__((ext_vector_type(2)));
#define LAS __attribute__((address_space(3)))
#define GAS __attribute__((address_space(1)))
#define DI __device__ __forceinline__
#define AI __attribute__((always_inline))
typedef __amdgpu_buffer_rsrc_t rsrc_t;

constexpr int SC1 = 16;
constexpr unsigned POISON = 0xFFFFFFFFu;
constexpr unsigned SPIN_CAP = 1u << 16;
constexpr int OOR = 0x7FFF0000;

constexpr int XB_SZ = 2048 * 640 * 2;
constexpr int KVSLOT = 8192;
constexpr int R_X = 0, R_O = XB_SZ, R_KV = 2 * XB_SZ;
constexpr int LSZ = 2 * XB_SZ + 256 * KVSLOT;
constexpr int WT_SZ = 153600, WO_SZ = 102400;
constexpr int OFF_WT = 4 * LSZ;
constexpr int OFF_CS = OFF_WT + 2 * 16 * WT_SZ;
constexpr int OFF_WO = OFF_CS + 32 * 10240;
constexpr int OFF_HB = OFF_WO + 2 * 8 * WO_SZ;
constexpr int OFF_CTRL = OFF_HB + 32768;
constexpr int F_X = 4096, F_R = F_X + 16384, F_H = F_R + 16384, F_A = F_H + 16384;
constexpr int OFF_DBG = OFF_CTRL + F_A + 16384;
constexpr int WS_USED = OFF_DBG + (1 << 20);

constexpr int L_Q = 102400, L_MISC = 153600;
constexpr int L_KV = L_MISC;
constexpr int L_ZERO = L_KV + 4032;
constexpr int L_DUMP = L_ZERO + 96;
constexpr int L_CS = L_DUMP + 16;
constexpr int L_ST = L_CS + 2048;
constexpr int L_GO = L_ST + 1024;
constexpr int LDS_BYTES = 163840;

#define OPQ(x) do { x = __builtin_amdgcn_readfirstlane(x); asm volatile("" : "+s"(x)); } while (0)
#define OPQV(x) asm volatile("" : "+v"(x))
#define PINA(x) OPQV(x)
#define SB() __builtin_amdgcn_sched_barrier(0)
#define DRAIN() asm volatile("s_waitcnt vmcnt(0)" ::: "memory")
#define VMWAIT(n) asm volatile("s_waitcnt vmcnt(%0)" :: "i"(n) : "memory")
#define NOUNROLL _Pragma("nounroll")
DI v4u ld128(rsrc_t rs, int voff, int soff) { return __builtin_amdgcn_raw_buffer_load_b128(rs, voff, soff, SC1); }
DI v2u ld64(rsrc_t rs, int voff, int soff) { return __builtin_amdgcn_raw_buffer_load_b64(rs, voff, soff, SC1); }
DI v4u ldi(rsrc_t rs, int& voff, int soff, int coff) { OPQV(voff); return ld128(rs, voff + (coff & 4095), soff + (coff & ~4095)); }
DI unsigned np(unsigned u) { return u < 0xFFFFFFFEu ? u : 0xFFFFFFFEu; }
DI void st128r(rsrc_t rs, int voff, int soff, v4u v) { __builtin_amdgcn_raw_buffer_store_b128(v, rs, voff, soff, SC1); asm volatile("s_nop 1" :: "v"(v)); }
DI void st128(rsrc_t rs, int voff, int soff, v4u v) { st128r(rs, voff, soff, (v4u){v.x, np(v.y), v.z, np(v.w)}); }
DI void st64(rsrc_t rs, int voff, int soff, v2u v) { v2u q = {v.x, np(v.y)}; __builtin_amdgcn_raw_buffer_store_b64(q, rs, voff, soff, SC1); }
DI void stp128(rsrc_t rs, int voff, int soff, v4u v) { __builtin_amdgcn_raw_buffer_store_b128(v, rs, voff, soff, 0); asm volatile("s_nop 1" :: "v"(v)); }
DI void poison128(rsrc_t rs, int voff, int soff) { st128r(rs, voff, soff, (v4u){POISON, POISON, POISON, POISON}); }
DI unsigned pk2(float a, float b) { f32x2 v = {a, b}; return __builtin_bit_cast(unsigned, __builtin_convertvector(v, bf16x2v)); }
DI float u2f(unsigned u) { return __builtin_bit_cast(float, u); }
DI unsigned f2u(float f) { return __builtin_bit_cast(unsigned, f); }
DI float rbf(float a) { return u2f(pk2(a, a) & 0xFFFF0000u); }
DI float blo(unsigned u) { return u2f(u << 16); }
DI float bhi(unsigned u) { return u2f(u & 0xFFFF0000u); }
DI float sum2(unsigned u, float acc) { return __builtin_amdgcn_fdot2_f32_bf16(__builtin_bit_cast(bf16x2v, u), __builtin_bit_cast(bf16x2v, 0x3F803F80u), acc, false); }
DI unsigned umax3(unsigned a, unsigned b, unsigned c) { return max(max(a, b), c); }
DI unsigned mx4(unsigned m, v4u v) { return umax3(m, v.y, v.w); }
DI bf16x8 tr2(LAS char* p, int off1, int off2) {
  s16x4 a = __builtin_amdgcn_ds_read_tr16_b64_v4i16((LAS s16x4*)(p + off1));
  s16x4 b = __builtin_amdgcn_ds_read_tr16_b64_v4i16((LAS s16x4*)(p + off2));
  return __builtin_shufflevector(a, b, 0, 1, 2, 3, 4, 5, 6, 7);
}
DI bf16x8 rd(LAS char* p, int off) { return *(LAS bf16x8*)(p + off); }
DI f32x16 mfma32(bf16x8 a, bf16x8 b, f32x16 c) { return __builtin_amdgcn_mfma_f32_32x32x16_bf16(a, b, c, 0, 0, 0); }
DI f32x4 mfma16(bf16x8 a, bf16x8 b, f32x4 c) { return __builtin_amdgcn_mfma_f32_16x16x32_bf16(a, b, c, 0, 0, 0); }
DI bf16x8 asb(v4u v) { return __builtin_bit_cast(bf16x8, v); }
DI float xsum32(float v) { unsigned u = f2u(v); auto r = __builtin_amdgcn_permlane32_swap(u, u, false, false); return u2f(r[0]) + u2f(r[1]); }
DI float xsum16(float v) { unsigned u = f2u(v); auto r = __builtin_amdgcn_permlane16_swap(u, u, false, false); return u2f(r[0]) + u2f(r[1]); }
DI int lane_id() { int l; asm volatile("v_mbcnt_lo_u32_b32 %0, -1, 0\n\tv_mbcnt_hi_u32_b32 %0, -1, %0" : "=v"(l)); return l; }
DI unsigned vzero() { unsigned z; asm volatile("v_mov_b32 %0, 0" : "=v"(z)); return z; }
DI unsigned vone() { unsigned z; asm volatile("v_mov_b32 %0, 0x3f80" : "=v"(z)); return z; }
DI void bar() { asm volatile("s_waitcnt lgkmcnt(0)" ::: "memory"); __builtin_amdgcn_s_barrier(); asm volatile("" ::: "memory"); }
#define MF16(acc, a, b) asm volatile("v_mfma_f32_16x16x32_bf16 %0, %1, %2, %0" : "+v"(acc) : "v"(a), "v"(b))
#define MFIN() asm volatile("s_nop 7\n\ts_nop 1" ::: "memory")
#define MF32(acc, a, b) do { acc = mfma32(a, b, acc); PINA(acc); SB(); } while (0)
DI void stw(LAS char* p, v4u& v) { *(LAS v4u*)p = v; }
#define RAWBAR() do { SB(); __builtin_amdgcn_s_barrier(); SB(); } while (0)
template <int I> struct IC { static constexpr int v = I; };
template <int B, int E, class F> DI void sfor(F&& f) { if constexpr (B < E) { f(IC<B>{}); sfor<B + 1, E>(f); } }

#define STAMP(k) do {} while (0)
#define CL 2
#define CSTAMP(k) do {} while (0)
#define DUMP()
#define ZST(i)
#define RCOUNT(k)
#define LSTAMP(k) do { STAMP(1 + lyr * 24 + (k)); if ((k) >= 2 && (k) < 8 && lyr == CL) CSTAMP(112 + (k)); } while (0)

struct Params {
  const unsigned short* x; const unsigned short* wqkv[2]; const unsigned short* wo[2]; const unsigned short* wphi;
  unsigned char* ws; unsigned short* out;
};
struct Ctx { rsrc_t rs; LAS char* L; int pi, pj, th, cs, w, lane; int xo, oo, ko; v2u ep; };

template <int N> DI unsigned chk128(const v4u* x) { unsigned m = 0;
#pragma unroll
  for (int k = 0; k < N; ++k) m = mx4(m, x[k]);
  return m; }
template <int N, int G0, int G1>
DI void fixup(const Ctx& c, v4u* x, unsigned mk, int voff, int soff, int stride, int kind) {
  if (__builtin_expect(__any(mk == POISON), 0)) {
    NOUNROLL for (unsigned spins = 0; spins < SPIN_CAP; ++spins) {
      OPQ(soff);
      bool bad[G1 - G0];
#pragma unroll
      for (int q = G0; q < G1; ++q) bad[q - G0] = __any(chk128<N>(x + N * q) == POISON);
      SB();
#pragma unroll
      for (int q = G0; q < G1; ++q) {
        if (bad[q - G0]) {
#pragma unroll
          for (int k = N * q; k < N * q + N; ++k) x[k] = ld128(c.rs, voff, soff + k * stride);
        }
      }
      if (!bad[0]) break;
      RCOUNT(kind);
    }
    __builtin_amdgcn_s_waitcnt(0x0F70);
  }
}

template <bool ISB>
DI void phi20(const float* v, float rs, unsigned* out) {
  const float rl = rs * 1.44269504088896f;
  if constexpr (!ISB) {
    float r[20];
#pragma unroll
    for (int i = 0; i < 20; ++i) {
      const float y = v[i] * rl;
      r[i] = fmaf(__builtin_amdgcn_fmed3f(y, 1.44269504e-6f, __builtin_inff()), 0.693147180559945f, __builtin_amdgcn_fmed3f(__builtin_amdgcn_exp2f(y), 0.f, 1.f));
    }
#pragma unroll
    for (int i = 0; i < 10; ++i) out[i] = pk2(r[2 * i], r[2 * i + 1]);
  } else {
    float mx = __builtin_amdgcn_fmed3f(v[0], v[1], __builtin_inff()), mn = __builtin_amdgcn_fmed3f(v[0], v[1], -__builtin_inff());
#pragma unroll
    for (int i = 2; i < 20; i += 2) { mx = fmaxf(fmaxf(mx, v[i]), v[i + 1]); mn = fminf(fminf(mn, v[i]), v[i + 1]); }
    const float cp = -mx * rl, cn = mn * rl;
    const f32x2 rl2 = {rl, rl}, cp2 = {cp, cp}, cn2 = {cn, cn};
    f32x2 ep[10], en[10], sp, sn;
#pragma unroll
    for (int i = 0; i < 10; ++i) {
      const f32x2 a = {v[2 * i], v[2 * i + 1]};
      const f32x2 yp = __builtin_elementwise_fma(a, rl2, cp2), yn = __builtin_elementwise_fma(a, -rl2, cn2);
      ep[i] = (f32x2){__builtin_amdgcn_exp2f(yp[0]), __builtin_amdgcn_exp2f(yp[1])};
      en[i] = (f32x2){__builtin_amdgcn_exp2f(yn[0]), __builtin_amdgcn_exp2f(yn[1])};
      sp = i ? sp + ep[i] : ep[i]; sn = i ? sn + en[i] : en[i];
    }
    const float ip = __builtin_amdgcn_rcpf(sp[0] + sp[1]), in = __builtin_amdgcn_rcpf(sn[0] + sn[1]);
#pragma unroll
    for (int i = 0; i < 10; ++i) { const f32x2 a = ep[i] * ip, b = en[i] * in; out[i] = pk2(a[0], a[1]); out[10 + i] = pk2(b[0], b[1]); }
  }
}
DI void swap16(float a, float b, float& lo, float& hi) { auto r = __builtin_amdgcn_permlane16_swap(f2u(a), f2u(b), false, false); lo = u2f(r[0]); hi = u2f(r[1]); }

template <int SL>
DI void poison_all(const Ctx& c, int lb, int n, int l16, int lane) {
  const int odd = c.cs & 1, s0 = (5 * c.cs + odd) >> 1;
  int xf = c.xo + (s0 * 2 + (c.w & 1)) * 1024, a = c.xo + (odd ? s0 - 1 : s0 + 2) * 2048 + odd * 512, b = c.oo + 1024 - 512, d = c.oo + 1024 + 20480 - 768;
  OPQ(xf); OPQ(a); OPQ(b); OPQ(d);
#define PZ() do { if (SL) __builtin_amdgcn_s_sleep(SL); } while (0)
  poison128(c.rs, l16, lb + xf); PZ(); poison128(c.rs, l16 + 1024, lb + xf); PZ();
  poison128(c.rs, l16, lb + c.oo); PZ(); poison128(c.rs, l16, lb + c.oo + 20480); PZ();
  poison128(c.rs, l16 + (lane < 32 ? a : (lane < 48 ? b : d)), lb); PZ();
  poison128(c.rs, l16, lb + c.ko + c.w * 1024); PZ();
  if (n > 6) poison128(c.rs, l16, lb + c.ko + c.w * 1024 + 4096);
}
DI int x_piece(const Ctx& c, int ct) { const int ch = 10 * c.cs + 2 * ct; return c.xo + (ch >> 2) * 2048 + (ch & 3) * 256; }
DI void uswap16(unsigned a, unsigned b, unsigned& lo, unsigned& hi) { auto r = __builtin_amdgcn_permlane16_swap(a, b, false, false); lo = r[0]; hi = r[1]; }
DI void x_chunks(const unsigned* xp, v4u* ch) {
#pragma unroll
  for (int k = 0; k < 3; ++k) {
    unsigned a0, a1, b0, b1;
    uswap16(xp[4 * k], xp[k < 2 ? 4 * k + 2 : 8], a0, b0); uswap16(xp[4 * k + 1], xp[k < 2 ? 4 * k + 3 : 9], a1, b1);
    ch[k] = (v4u){a0, a1, b0, b1};
  }
}
DI int x_voff(const Ctx& c, int lane) { int d = (c.cs & 1) ? 1536 : 512; OPQ(d); return ((lane >> 4) & 1) * d + (lane >> 5) * 256 + (lane & 15) * 16; }
DI void publish_x(const Ctx& c, int lb, int xv, int xv4, const v4u* ch) {
#pragma unroll
  for (int k = 0; k < 3; ++k) st128(c.rs, k < 2 ? xv : xv4, lb + x_piece(c, 2 * k), ch[k]);
}
DI void publish_stats(const Ctx& c, int lb, int lane, const unsigned* xp) {
  float s = 0.f;
#pragma unroll
  for (int i = 0; i < 10; ++i) s = sum2(xp[i], s);
  s = xsum32(xsum16(s));
  const float m = s * (1.f / 80.f);
  f32x2 q2 = {0.f, 0.f};
#pragma unroll
  for (int i = 0; i < 10; ++i) { const f32x2 d = (f32x2){blo(xp[i]), bhi(xp[i])} - m; q2 = __builtin_elementwise_fma(d, d, q2); }
  const float q = xsum32(xsum16(q2[0] + q2[1]));
  st64(c.rs, lane < 16 ? lane * 8 : OOR, lb + c.ko + c.w * 128, (v2u){f2u(s), f2u(q)});
}
#define HINT 0
#define PD3 0
#define PS 1
#define OPF 10
#define PD1 4
#define PD2 4
DI v2u next_epoch(v2u e) { unsigned x = __builtin_amdgcn_readfirstlane(e.x) + 1u, y = __builtin_amdgcn_readfirstlane(e.y) + (x == 0u); return (v2u){x, y}; }
DI void wait_flags(const Ctx& c, int voff, int soff, int kind) {
  NOUNROLL for (unsigned spins = 0; spins < SPIN_CAP; ++spins) {
    const v2u f = ld64(c.rs, voff, OFF_CTRL + soff);
    if (__all(f.x == c.ep.x && f.y == c.ep.y)) break;
    RCOUNT(kind);
    __builtin_amdgcn_s_sleep(2);
  }
  asm volatile("" ::: "memory");
}
DI void set_flag(const Ctx& c, int voff) { __builtin_amdgcn_raw_buffer_store_b64(c.ep, c.rs, voff, OFF_CTRL, SC1); }
#define PW0 8
#define PW1 8
#define HPRIO 0
#define HVE 0
#define PPOS 0
#define PSL 0
#define XRA 10
#define XRN 12
#define XE 4
#define PAN 8
#define XPN 16
#define RQ 3
#define B4N 69
template <bool ISB>
DI void layer_c(const Ctx& c, const Params& p, const int lyr) {
  unsigned xp[10]; int xrv, xr4; v4u xc[3];
  int lane = lane_id();
  const int w = c.w, t32 = lane & 31, hh = lane >> 5, t16 = lane & 15, g = lane >> 4;
  constexpr int NQ = ISB ? 10 : 5, NKS = ISB ? 5 : 3, TRS = ISB ? 160 : 96;
  constexpr int type = ISB ? 1 : 0;
  LAS char* L = c.L;
  int l16 = lane * 16;
  LAS char* wa = L + l16;
  const int lb = __builtin_amdgcn_readfirstlane(lyr * LSZ);
  v4u xf[40];
  const int xs = lb + R_X + (c.pi * 4 + w) * 40960;
  const int q4 = (lane & 15) >> 2, p4 = lane & 3;
  LAS char* wh = wa + 51200;
  constexpr int VO = ISB ? 80 : 48;
  LAS char* dst = L + hh * 20480 + (32 * w + t32) * TRS;
  LAS char* ta = L + (w & 1) * 20480 + (4 * g + q4) * TRS + 8 * p4;
  LAS char* tb = ta + VO + 32 * (w >> 1) - (((w >> 1) && p4 >= 2) ? 16 : 0);
  int kvv = 16 * (w >> 1) + t16 < 21 ? g * 336 + (16 * (w >> 1) + t16) * 16 : OOR;
  int kvw = g < NQ % 4 ? kvv : OOR;
  LAS char* csa = L + (g ? L_ZERO : L_CS + type * 1024 + t16 * 64);
  LAS char* wq = L + L_ST + w * 256 + t16 * 8;
  LAS char* rsa = L + L_ST + w * 256 + t32 * 8 + 4;
  OPQV(wh); OPQV(dst); OPQV(ta); OPQV(tb); OPQV(kvv); OPQV(kvw); OPQV(csa); OPQV(wq); OPQV(rsa);
  SB();
  if (ISB || lyr) { VMWAIT(1); __builtin_amdgcn_s_sleep(PD1); }
  LSTAMP(1);
  constexpr int XP = XPN, XR = ISB ? XRN : XRA;
#pragma unroll
  for (int k = 0; k < XP; ++k) { xf[k] = ldi(c.rs, l16, xs, k * 1024); SB(); }
  if (!ISB) VMWAIT(XP);
  RAWBAR();
  LSTAMP(2);
  f32x4 a[5][2] = {};
  constexpr int PA = PAN;
  bf16x8 A[PA];
#pragma unroll
  for (int t = 0; t < 5; ++t) { OPQV(a[t][0]); OPQV(a[t][1]); }
  SB();
#pragma unroll
  for (int n = 0; n < PA; ++n) A[n] = rd(wa, n * 1024);
  SB();
  float rs = 0.f; v4u cs[4], mlo[2], mhi[2]; unsigned mm[2];
  LAS char* wq2;
  unsigned mk = chk128<4>(xf);
  sfor<0, 100>([&](auto N) AI {
    constexpr int n = decltype(N)::v, s = n / 5, t = n % 5, gq = n / 10, u = n % 10;
    if constexpr (u == 0) { fixup<4, gq, (XP / 4 + gq < 10 ? XP / 4 + gq : 10)>(c, xf, mk, l16, xs, 1024, 0); SB(); }
    if constexpr ((n & 1) == 0) { OPQV(A[(n + 1) % PA]); SB(); }
    MF16(a[t][0], A[n % PA], asb(xf[2 * s])); SB();
    if constexpr ((u == 0 || u == 2 || u == 4 || u == 8) && XP + 4 * gq + (u == 8 ? 3 : u / 2) < 40) { constexpr int k = XP + 4 * gq + (u == 8 ? 3 : u / 2); xf[k] = ldi(c.rs, l16, xs, k * 1024); }
    if constexpr (gq < 9 && (u == 5 || u == 7)) { mk = mx4(u == 5 ? 0u : mk, xf[4 * gq + u]); OPQV(mk); }
    if constexpr (n == 70) { mm[0] = *(LAS unsigned*)(wq); mm[1] = *(LAS unsigned*)(wq + 128); }
    if constexpr (n == 71) { rs = *(LAS float*)(rsa); }
    if constexpr (n == 72) { cs[0] = *(LAS v4u*)(csa); }
    if constexpr (n == 73) { cs[1] = *(LAS v4u*)(csa + 16); }
    if constexpr (n == 74) { cs[2] = *(LAS v4u*)(csa + 32); }
    if constexpr (n == 84 || n == 85) { mlo[n - 84].z = vzero(); mlo[n - 84].w = vzero(); }
    if constexpr (n == 86 || n == 87) { mhi[n - 86].x = vzero(); mhi[n - 86].y = vzero(); }
    if constexpr (n == 88) { OPQV(mlo[0]); OPQV(mlo[1]); OPQV(mhi[0]); OPQV(mhi[1]); }
    if constexpr (n == 80 || n == 81) { mlo[n - 80].x = mm[n - 80]; mlo[n - 80].y = mm[n - 80]; }
    if constexpr (n == 82 || n == 83) { mhi[n - 82].z = mm[n - 82]; mhi[n - 82].w = mm[n - 82]; }
    SB();
    MF16(a[t][1], A[n % PA], asb(xf[2 * s + 1])); SB();
    if constexpr (n + PA < 100) A[n % PA] = (n + PA < 50) ? rd(wa, (n + PA) * 1024) : rd(wh, (n + PA - 50) * 1024);
    if constexpr (gq < 9 && (u == 6 || u == 8)) { mk = mx4(mk, xf[4 * gq + u - 2]); OPQV(mk); }
    SB();
    if constexpr (n >= 90 && n < 95) {
      constexpr int q = (n + 2) % 5;
      MF16(a[q][0], asb(cs[q >> 1]), asb((q & 1) ? mhi[0] : mlo[0])); SB();
      MF16(a[q][1], asb(cs[q >> 1]), asb((q & 1) ? mhi[1] : mlo[1])); SB();
    }
    if constexpr (n == B4N) RAWBAR();
  });
  MFIN();
#pragma unroll
  for (int t = 0; t < 5; ++t) { OPQV(a[t][0]); OPQV(a[t][1]); }
  SB();
  LSTAMP(3);
  {
    float kx[20], vx[20];
#pragma unroll
    for (int i = 0; i < 20; ++i) swap16(a[i >> 2][0][i & 3], a[i >> 2][1][i & 3], kx[i], vx[i]);
    unsigned v[10], o[ISB ? 20 : 10];
#pragma unroll
    for (int i = 0; i < 10; ++i) v[i] = pk2(vx[2 * i] * rs, vx[2 * i + 1] * rs);
    *(LAS v4u*)(dst + VO) = (v4u){v[0], v[1], v[2], v[3]}; *(LAS v4u*)(dst + VO + 16) = (v4u){v[4], v[5], v[6], v[7]};
    *(LAS v4u*)(dst + VO + 32) = (v4u){v[8], v[9], vone(), vzero()};
    phi20<ISB>(kx, rs, o);
#pragma unroll
    for (int i = 0; i < (ISB ? 5 : 2); ++i) *(LAS v4u*)(dst + 16 * i) = (v4u){o[4 * i], o[4 * i + 1], o[4 * i + 2], o[4 * i + 3]};
    if (!ISB) *(LAS v2u*)(dst + 32) = (v2u){o[8], o[9]};
  }
  SB();
  bar();
  LSTAMP(4);
  {
    constexpr int NF = ISB ? 3 : 2;
    bf16x8 fa[4][NF], fb[4];
    f32x4 kv[NF] = {};
#pragma unroll
    for (int ks = 0; ks < 6; ++ks) {
      if (ks < 4) {
        fb[ks] = tr2(tb, 32 * ks * TRS, (32 * ks + 16) * TRS);
#pragma unroll
        for (int ft = 0; ft < NF; ++ft) fa[ks][ft] = tr2(ta, 32 * ks * TRS + 32 * ft, (32 * ks + 16) * TRS + 32 * ft);
        SB();
      }
      if (ks >= 1 && ks - 1 < XE && ks - 1 < XR) { xf[ks - 1] = ldi(c.rs, l16, xs, (ks - 1) * 1024); SB(); }
      if (ks >= 2) {
#pragma unroll
        for (int ft = NF - 1; ft >= 0; --ft) { kv[ft] = mfma16(fa[ks - 2][ft], fb[ks - 2], kv[ft]); OPQV(kv[ft]); }
        SB();
      }
    }
    const int kvs = lb + c.ko + 512 + (w & 1) * NQ * 336;
    wq = wa + L_Q; wq2 = L + L_Q + 2048 + g * 128 + (t16 >> 3) * 64 + (t16 & 3) * 16;
    xrv = x_voff(c, lane); xr4 = (g & 1) ? OOR : xrv;
    OPQV(wq); OPQV(wq2); OPQV(xrv); OPQV(xr4);
    SB();
#pragma unroll
    for (int ft = NF - 1; ft >= 0; --ft) st128(c.rs, (ft < NF - 1 ? kvv : kvw) + ft * 1344, kvs, __builtin_bit_cast(v4u, kv[ft]));
  }
  RAWBAR();
  LSTAMP(5);
  int k0, k1, ov, xov;
  unsigned pq[NKS * 4];
  {
    f32x4 q[3][2] = {};
#pragma unroll
    for (int t = 0; t < 3; ++t) { OPQV(q[t][0]); OPQV(q[t][1]); }
    SB();
    auto rdq = [&](int n) AI { const int s = (n / 3 + XR / 2) % 20, t = n % 3; return t < 2 ? rd(wq, s * 2560 + t * 1024) : rd(wq2, s * 2560); };
#pragma unroll
    for (int n = 0; n < PA; ++n) A[n] = rdq(n);
    cs[2] = *(LAS v4u*)(csa + 32); cs[3] = *(LAS v4u*)(csa + 48);
    OPQV(mm[0]); OPQV(mm[1]);
#pragma unroll
    for (int h = 0; h < 2; ++h) { mlo[h] = (v4u){mm[h], mm[h], vzero(), vzero()}; mhi[h] = (v4u){vzero(), vzero(), mm[h], mm[h]}; OPQV(mlo[h]); OPQV(mhi[h]); }
    SB();
    sfor<0, 60>([&](auto N) AI {
      constexpr int n = decltype(N)::v, s = (n / 3 + XR / 2) % 20, t = n % 3;
      if constexpr ((n & 1) == 0) { OPQV(A[(n + 1) % PA]); SB(); }
      MF16(q[t][0], A[n % PA], asb(xf[2 * s])); SB();
      if constexpr (n % RQ == 0 && XE + n / RQ < XR) xf[XE + n / RQ] = ldi(c.rs, l16, xs, (XE + n / RQ) * 1024);
      if constexpr (n == 41 || n == 44 || n == 47) { constexpr int k = (n - 41) / 3; xc[k] = ld128(c.rs, k < 2 ? xrv : xr4, lb + x_piece(c, 2 * k)); }
      SB();
      MF16(q[t][1], A[n % PA], asb(xf[2 * s + 1])); SB();
      if constexpr (n + PA < 60) A[n % PA] = rdq(n + PA);
      SB();
      if constexpr (n >= 6 && n < 9) {
        constexpr int z = (n + 1) % 3;
        MF16(q[z][0], asb(cs[(5 + z) >> 1]), asb((z & 1) ? mlo[0] : mhi[0]));
        MF16(q[z][1], asb(cs[(5 + z) >> 1]), asb((z & 1) ? mlo[1] : mhi[1]));
        SB();
      }
    });
    MFIN();
#pragma unroll
    for (int t = 0; t < 3; ++t) { OPQV(q[t][0]); OPQV(q[t][1]); }
    SB();
    LSTAMP(6);
    {
      const int e0 = 4 * (t32 >> 3) + (t32 & 3), e1 = min(16 + e0, 20);
      const bool own = ((t32 >> 2) & 1) == hh;
      k0 = own ? L_KV + hh * 2016 + e0 * 96 : L_ZERO; k1 = own ? L_KV + hh * 2016 + e1 * 96 : L_ZERO;
      xov = (t32 >> 4) * 20480 + t16 * 16; ov = xov + 768 * hh; xov = hh ? OOR : xov;
      OPQV(k0); OPQV(k1); OPQV(ov); OPQV(xov);
    }
    float qx[24];
#pragma unroll
    for (int i = 0; i < 12; ++i) swap16(q[i >> 2][0][i & 3], q[i >> 2][1][i & 3], qx[i], qx[12 + i]);
    phi20<ISB>(qx, rs, pq);
#pragma unroll
    for (int i = 0; i < (ISB ? 20 : 10); ++i) OPQV(pq[i]);
    if (!ISB) { pq[10] = vzero(); pq[11] = vzero(); }
  }
  f32x4 acc[5];
  {
    unsigned d0, d1;
#pragma unroll
    for (int k = 0; k < 2; ++k) { uswap16(xc[k].x, xc[k].z, xp[4 * k], xp[4 * k + 2]); uswap16(xc[k].y, xc[k].w, xp[4 * k + 1], xp[4 * k + 3]); }
    uswap16(xc[2].x, xc[2].z, xp[8], d0); uswap16(xc[2].y, xc[2].w, xp[9], d1);
  }
#pragma unroll
  for (int ct = 0; ct < 5; ++ct) { acc[ct] = (f32x4){blo(xp[2 * ct]), bhi(xp[2 * ct]), blo(xp[2 * ct + 1]), bhi(xp[2 * ct + 1])}; OPQV(acc[ct]); }
  LSTAMP(7);
  RAWBAR();
  LSTAMP(8);
  {
    bf16x8 f0[NKS], f1[NKS];
#pragma unroll
    for (int ks = 0; ks < NKS; ++ks) f1[ks] = rd(L + k1, 16 * ks);
#pragma unroll
    for (int ks = 0; ks < NKS; ++ks) f0[ks] = rd(L + k0, 16 * ks);
    SB();
    f32x16 n0 = {}, n1 = {};
#pragma unroll
    for (int ks = 0; ks < NKS; ++ks) MF32(n1, f1[ks], asb((v4u){pq[4 * ks], pq[4 * ks + 1], pq[4 * ks + 2], pq[4 * ks + 3]}));
    float id = 0.f;
    const int os = lb + c.oo;
#pragma unroll
    for (int ks = 0; ks < NKS; ++ks) {
      MF32(n0, f0[ks], asb((v4u){pq[4 * ks], pq[4 * ks + 1], pq[4 * ks + 2], pq[4 * ks + 3]}));
      if (ks == NKS - 2) { OPQV(n1); id = __builtin_amdgcn_rcpf(n1[4] + 1e-6f); OPQV(id); }
      if (ks == NKS - 1) {
        const unsigned m0 = pk2(n1[0] * id, n1[1] * id), m1 = pk2(n1[2] * id, n1[3] * id);
        auto r0 = __builtin_amdgcn_permlane32_swap(m0, m0, false, false); auto r1 = __builtin_amdgcn_permlane32_swap(m1, m1, false, false);
        st128(c.rs, xov + 512, os, (v4u){r0[0], r1[0], r0[1], r1[1]});
      }
      SB();
    }
    unsigned d[8];
#pragma unroll
    for (int i = 0; i < 8; ++i) d[i] = pk2(n0[2 * i] * id, n0[2 * i + 1] * id);
    st128(c.rs, ov, os, (v4u){d[0], d[1], d[2], d[3]});
    st128(c.rs, ov + 256, os, (v4u){d[4], d[5], d[6], d[7]});
  }
  RAWBAR();
  LSTAMP(9);
  constexpr int WPD = 32;
  bf16x8 wf[WPD];
#pragma unroll
  for (int k = 0; k < WPD; ++k) wf[k] = rd(wa, k * 1024);
  SB();
  VMWAIT(0);
  __builtin_amdgcn_s_sleep(PD2);
  LSTAMP(10);
  v4u of[20];
  const int oos = lb + R_O + (c.pi * 8 + 4 * c.th + w) * 20480;
#pragma unroll
  for (int s = 0; s < OPF; ++s) { of[s] = ldi(c.rs, l16, oos, s * 1024); SB(); }
  mk = chk128<5>(of);
  sfor<0, 100>([&](auto N) AI {
    constexpr int n = decltype(N)::v, s = n / 5, ct = n % 5, gq = n / 25, u = n % 25;
    if constexpr (u == 0) { fixup<5, gq, ((OPF + 6 * gq) / 5 < 4 ? (OPF + 6 * gq) / 5 : 4)>(c, of, mk, l16, oos, 1024, 2); SB(); }
    if constexpr (n >= WPD && (n & 3) == 0) { OPQV(wf[(n + 3) % WPD]); SB(); }
    MF16(acc[ct], wf[n % WPD], asb(of[s])); SB();
    if constexpr (n % 4 != 3) { constexpr int m = WPD + 3 * (n / 4) + n % 4; if constexpr (m < 100) wf[m % WPD] = (m < 50) ? rd(wa, m * 1024) : rd(wh, (m - 50) * 1024); }
    if constexpr (n % 4 == 3 && OPF + n / 4 < 20) of[OPF + n / 4] = ldi(c.rs, l16, oos, (OPF + n / 4) * 1024);
    if constexpr (gq < 3 && u >= 14 && u < 24 && (u & 1) == 0) { constexpr int q = (u - 14) / 2; mk = mx4(q == 0 ? 0u : mk, of[5 * gq + 5 + q]); OPQV(mk); }
    SB();
  });
  MFIN();
#pragma unroll
  for (int t = 0; t < 5; ++t) OPQV(acc[t]);
  SB();
  LSTAMP(11);
#pragma unroll
  for (int ct = 0; ct < 5; ++ct) { xp[2 * ct] = pk2(acc[ct][0], acc[ct][1]); xp[2 * ct + 1] = pk2(acc[ct][2], acc[ct][3]); }
  x_chunks(xp, xc);
  if (lyr < 3) {
    publish_x(c, lb + LSZ, xrv, xr4, xc);
    RAWBAR();
    publish_stats(c, lb + LSZ, lane, xp);
  } else {
    unsigned short* o = p.out + (size_t)(128 * c.pi + 64 * c.th + 16 * w + t16) * 640 + 80 * c.cs + 16 * (g & 1) + 8 * (g >> 1);
    *(GAS v4u*)o = xc[0]; *(GAS v4u*)(o + 32) = xc[1];
    if (!(g & 1)) *(GAS v4u*)(o + 64) = xc[2];
  }
  SB();
  LSTAMP(0);
}

#define KM 55
#define Q1 10
#define SL1 1
#define Q2 20
#define SL2 0
#define Q3 20
#define Q4 25
#define WE 8
#define Q5 40
#define HS 10
#define SL3 0
template <bool ISB>
DI void layer_h(const Ctx& c, const int lyr) {
  int lane = lane_id();
  const int w = c.w, t32 = lane & 31, hh = lane >> 5;
  constexpr int NQ = ISB ? 10 : 5, NP = 2 * NQ * 21;
  constexpr int type = ISB ? 1 : 0;
  LAS char* L = c.L;
  int l16 = lane * 16;
  LAS char* wa = L + l16;
  LAS char* sd = wa + w * 25600;
  const int lb = __builtin_amdgcn_readfirstlane(lyr * LSZ);
  int wos = OFF_WO + (type * 8 + c.cs) * WO_SZ + w * 25600;
  int wt0 = OFF_WT + ((1 - type) * 16 + c.pj) * WT_SZ;
  if (lyr == 3) wt0 = OOR - 0x1000000;
  OPQ(wos); OPQ(wt0);
  bar();
  if (!ISB && lyr == 0) {
    *(volatile LAS unsigned*)(L + L_GO + 8) = ~c.ep.x;
    DRAIN();
    int f0 = F_H + ((c.pj * 4 + w) * 16 + c.pi) * 16, f1 = F_R + ((c.pi * 4 + w) * 16 + c.pj) * 16; OPQ(f0); OPQ(f1);
    set_flag(c, lane == 0 ? f0 : (lane == 1 ? f1 : OOR));
    if (w >= 2) wait_flags(c, min(lane, 19) * 32 + 16, F_A + c.pj * 1024, 6);
  }
  {
    v2u stv[4];
    const int sv = hh * (4 * 16 * KVSLOT) + t32 * 8; int ss = lb + R_KV + ((w >> 1) * 128 + c.pi) * KVSLOT + (w & 1) * 256;
    NOUNROLL for (unsigned spins = 0; spins < SPIN_CAP; ++spins) {
#pragma unroll
      for (int q = 0; q < 4; ++q) stv[q] = ld64(c.rs, sv, ss + q * 16 * KVSLOT);
      if (!__any(max(umax3(stv[0].y, stv[1].y, stv[2].y), stv[3].y) == POISON)) break;
      RCOUNT(3);
      __builtin_amdgcn_s_sleep(4);
    }
    const float mean = xsum32((u2f(stv[0].x) + u2f(stv[1].x)) + (u2f(stv[2].x) + u2f(stv[3].x))) * (1.f / 640.f);
    float m2 = 0.f;
#pragma unroll
    for (int q = 0; q < 4; ++q) { const float d = u2f(stv[q].x) * (1.f / 80.f) - mean; m2 += fmaf(80.f * d, d, u2f(stv[q].y)); }
    const float var = rbf(xsum32(m2) * (1.f / 640.f));
    const float rs = __builtin_amdgcn_rcpf(rbf(__builtin_amdgcn_sqrtf(rbf(var + 1e-5f))));
    *(LAS v2u*)(L + L_ST + w * 256 + t32 * 8) = (v2u){pk2(-mean, -mean), f2u(rs)};
  }
  auto colsum = [&](int t) AI {
    const int i = (w & 1) * 64 + lane;
    unsigned cs[20];
#pragma unroll
    for (int k = 0; k < 20; ++k) cs[k] = __builtin_amdgcn_raw_buffer_load_b32(c.rs, i * 4 + (k & 7) * 512, OFF_CS + (t * 16 + c.pj) * 10240 + (k >> 3) * 4096, SC1);
    float s = 0.f;
#pragma unroll
    for (int k = 0; k < 20; ++k) s += u2f(cs[k]);
    const float hi = rbf(s), mid = rbf(s - hi), lo = rbf(s - hi - mid);
    *(LAS v2u*)(L + L_CS + t * 1024 + i * 8) = (v2u){pk2(hi, mid), pk2(lo, 0.f)};
  };
  if (lyr == (int)ISB && w >= 2) colsum(type);
  if (!ISB && lyr == 0) {
    v4u u0[13];
#pragma unroll
    for (int k = 0; k < 13; ++k) u0[k] = ldi(c.rs, l16, OFF_WT + c.pj * WT_SZ + w * 12800, 102400 + k * 1024);
    LAS char* ud = wa + 102400 + w * 12800;
#pragma unroll
    for (int k = 0; k < 13; ++k) stw(k < 12 ? ud + k * 1024 : (hh ? L + L_DUMP : ud + 12 * 1024), u0[k]);
  }
  constexpr int NR = ISB ? 2 : 1, PW = NP / 4 + (ISB ? 0 : 1);
  int gv[NR], kvl[NR];
#pragma unroll
  for (int r = 0; r < NR; ++r) {
    const int pn = lane + 64 * r, pc = w * PW + pn; const bool ok = pn < PW && pc < NP;
    const int hf = (pc * 3121) >> 16, zi = w * 11 + lane - 53;
    gv[r] = ok ? pc * 16 : OOR;
    kvl[r] = ok ? L_KV + (pc - hf * 21) * 96 + hf * 8 + (hf >= NQ ? 2016 - NQ * 8 : 0) : ((!ISB && lane >= 53 && zi < 42) ? L_KV + zi * 96 + 40 : L_DUMP);
    OPQV(gv[r]); OPQV(kvl[r]);
  }
  v4u wst[25], up[13], nxt[25];
  auto bg = [&](int n) AI {
    if (n < 25) wst[n] = ldi(c.rs, l16, wos, n * 1024);
    else if (n < 38) up[n - 25] = ldi(c.rs, l16, wt0 + w * 12800, 102400 + (n - 25) * 1024);
    else if (n < 63) nxt[n - 38] = ldi(c.rs, l16, wt0 + w * 25600, (n - 38) * 1024);
    SB();
  };
  if (ISB || lyr) {
#pragma unroll
    for (int n = 0; n < Q1; ++n) { bg(n); __builtin_amdgcn_s_sleep(SL1); }
  }
  bar();
  const unsigned t4 = (unsigned)__builtin_amdgcn_s_memrealtime();
  const int hb = OFF_HB + lyr * 8192;
  if (w == 0) __builtin_amdgcn_raw_buffer_store_b64((v2u){t4, c.ep.x}, c.rs, lane == 0 ? 0 : OOR, hb + (c.pi * 16 + c.pj) * 8, SC1);
  if (!ISB && lyr == 0) {
    wait_flags(c, l16, F_H + (c.pj & 7) * 1024, 7);
    if (w < 2) wait_flags(c, l16, w ? F_H + (c.pj | 8) * 1024 : F_R + c.pi * 1024, 7);
#pragma unroll
    for (int n = 0; n < Q1; ++n) bg(n);
  }
  if (lyr < 2) poison_all<PSL>(c, lb + 2 * LSZ, ISB ? 7 : 6, l16, lane);
#pragma unroll
  for (int n = Q1; n < Q2; ++n) { bg(n); __builtin_amdgcn_s_sleep(SL2); }
  v4u hv[2];
  const int hvo = w == 3 ? l16 : OOR;
  RAWBAR();
#pragma unroll
  for (int q = 0; q < 2; ++q) hv[q] = ld128(c.rs, hvo, hb + q * 1024);
#pragma unroll
  for (int n = Q2; n < Q3; ++n) bg(n);
  RAWBAR();
  const unsigned t6 = (unsigned)__builtin_amdgcn_s_memrealtime();
  v4u kg[NR][16];
  const int gs = lb + R_KV + c.pj * 16 * KVSLOT + 512;
  {
    volatile LAS unsigned* go = (volatile LAS unsigned*)(L + L_GO) + (lyr & 1);
    int lag = 0;
    if (w == 3) {
      NOUNROLL for (unsigned spins = 0; spins < 64; ++spins) {
        bool ok = true; lag = 0;
#pragma unroll
        for (int q = 0; q < 2; ++q) { ok &= hv[q].y == c.ep.x && hv[q].w == c.ep.x; lag = max(max(lag, (int)(hv[q].x - t4)), (int)(hv[q].z - t4)); }
        if (__all(ok)) break;
        RCOUNT(5);
#pragma unroll
        for (int q = 0; q < 2; ++q) hv[q] = ld128(c.rs, hvo, hb + q * 1024);
        lag = 0;
      }
#pragma unroll
      for (int d = 32; d >= 1; d >>= 1) lag = max(lag, __shfl_xor(lag, d));
      lag = min(lag, 400);
      *go = ((unsigned)(lyr + 1) << 16) | (unsigned)lag;
    }
#pragma unroll
  for (int k = 0; k < WE; ++k) { if (Q3 + k < Q4) bg(Q3 + k); stw(sd + k * 1024, wst[k]); SB(); }
    if (w != 3) {
      unsigned v = 0;
      NOUNROLL for (unsigned spins = 0; spins < 4096; ++spins) { v = *go; if ((v >> 16) == (unsigned)(lyr + 1)) break; __builtin_amdgcn_s_sleep(1); }
      lag = v & 0x1FF;
    }
    lag = __builtin_amdgcn_readfirstlane(lag) + KM;
    NOUNROLL while ((int)((unsigned)__builtin_amdgcn_s_memrealtime() - t6) < lag) __builtin_amdgcn_s_sleep(1);
  }
#pragma unroll
  for (int n = 0; n < 16 * NR; ++n) { kg[n % NR][n / NR] = ld128(c.rs, gv[n % NR], gs + (n / NR) * KVSLOT); SB(); }
  {
    f32x4 sum[NR];
    bool redo = false;
#pragma unroll
    for (int i = 0; i < 16; ++i) {
      unsigned m = 0;
#pragma unroll
      for (int r = 0; r < NR; ++r) { m = mx4(m, kg[r][i]); sum[r] = i ? sum[r] + __builtin_bit_cast(f32x4, kg[r][i]) : __builtin_bit_cast(f32x4, kg[r][i]); OPQV(sum[r]); }
      if (__builtin_expect(__any(m == POISON), 0)) {
        redo = true;
        int gs2 = gs; OPQ(gs2);
#pragma unroll
        for (int r = 0; r < NR; ++r) kg[r][i] = ld128(c.rs, gv[r], gs2 + i * KVSLOT);
      }
    }
    if (__builtin_expect(redo, 0)) {
      NOUNROLL for (unsigned spins = 0; spins < SPIN_CAP; ++spins) {
        RCOUNT(1);
        bool any = false;
        int gs2 = gs; OPQ(gs2);
        __builtin_amdgcn_s_waitcnt(0x0F70);
#pragma unroll
        for (int i = 0; i < 16; ++i)
#pragma unroll
          for (int r = 0; r < NR; ++r) sum[r] = i ? sum[r] + __builtin_bit_cast(f32x4, kg[r][i]) : __builtin_bit_cast(f32x4, kg[r][i]);
        float t = sum[0][1] + sum[0][3];
        if (NR == 2) t += sum[NR - 1][1] + sum[NR - 1][3];
        if (!__any(t != t)) break;
#pragma unroll
        for (int i = 0; i < 16; ++i) {
          unsigned m = 0;
#pragma unroll
          for (int r = 0; r < NR; ++r) m = mx4(m, kg[r][i]);
          if (__any(m == POISON)) {
            any = true;
#pragma unroll
            for (int r = 0; r < NR; ++r) kg[r][i] = ld128(c.rs, gv[r], gs2 + i * KVSLOT);
          }
        }
        if (!any) break;
      }
      __builtin_amdgcn_s_waitcnt(0x0F70);
    }
#pragma unroll
    for (int r = 0; r < NR; ++r) *(LAS v2u*)(L + kvl[r]) = (v2u){pk2(sum[r][0], sum[r][1]), pk2(sum[r][2], sum[r][3])};
  }
  SB();
  bar();
#pragma unroll
  for (int k = WE; k < 25; ++k) stw(sd + k * 1024, wst[k]);
  SB();
  bar();
  if (lyr == 3) return;
#pragma unroll
  for (int n = Q4; n < Q5; ++n) bg(n);
  {
    LAS char* ud = wa + 102400 + w * 12800;
    LAS char* ul = hh ? L + L_DUMP : ud + 12 * 1024;
#pragma unroll
    for (int k = 0; k < 13; ++k) stw(k < 12 ? ud + k * 1024 : ul, up[k]);
  }
  SB();
  __builtin_amdgcn_s_sleep(HS);
#pragma unroll
  for (int n = Q5; n < 63; ++n) { bg(n); __builtin_amdgcn_s_sleep(SL3); }
  RAWBAR();
#pragma unroll
  for (int k = 0; k < 25; ++k) stw(sd + k * 1024, nxt[k]);
  SB();
}

DI void set_ctx(Ctx& c, const Params& p, int b, char* smem, int wv = -1) {
  c.L = (LAS char*)smem;
  c.rs = __builtin_amdgcn_make_buffer_rsrc(p.ws, 0, WS_USED, 0x00020000);
  const int xcd = b & 7, r = b >> 3;
  c.pi = 4 * (xcd >> 1) + (r & 3); c.pj = 8 * (xcd & 1) + (r >> 2); c.th = c.pj >> 3; c.cs = c.pj & 7;
  c.lane = threadIdx.x & 63; c.w = wv >= 0 ? wv : (__builtin_amdgcn_readfirstlane(threadIdx.x >> 6) & 3);
  c.xo = R_X + (4 * c.pi + 2 * c.th + (c.w >> 1)) * 40960 + (c.w & 1) * 1024;
  c.oo = R_O + (c.pi * 8 + 2 * c.w) * 20480 + 5 * c.pj * 256;
  c.ko = R_KV + (c.pj * 16 + c.pi) * KVSLOT;
  OPQ(c.xo); OPQ(c.oo); OPQ(c.ko);
}
constexpr int CODE_KB = 48;
__global__ void __launch_bounds__(512, 2) mega(Params p) {
  extern __shared__ __attribute__((aligned(16))) char smem[];
  unsigned pf;
  {
    unsigned long long pc; asm volatile("s_getpc_b64 %0" : "=s"(pc));
    const unsigned ln = min(((blockIdx.x >> 3) & 3u) * 128u + (threadIdx.x >> 2), (unsigned)(CODE_KB * 8 - 1));
    asm volatile("global_load_dword %0, %1, %2" : "=v"(pf) : "v"(ln * 128u), "s"(pc & ~127ull) : "memory");
  }
  Ctx c;
  set_ctx(c, p, blockIdx.x, smem);
  const int w8 = __builtin_amdgcn_readfirstlane(threadIdx.x >> 6);
  LAS char* L = c.L;
  const int lane = c.lane, w = c.w, t16 = lane & 15, g = lane >> 4, q4 = t16 >> 2, p4 = lane & 3;
  int l16 = lane * 16;
  { const int i_ = threadIdx.x; ZST(i_); }
  v2u ep;
  if (w8 < 4) {
    STAMP(0); CSTAMP(120);
    const rsrc_t rx = __builtin_amdgcn_make_buffer_rsrc((void*)p.x, 0, 2048 * 1280, 0x00020000);
    const int xa = t16 * 1280 + 32 * (g & 1) + 16 * (g >> 1);
    v4u xc[3];
#pragma unroll
    for (int k = 0; k < 3; ++k) xc[k] = __builtin_amdgcn_raw_buffer_load_b128(rx, (k == 2 && (g & 1)) ? OOR : xa + 64 * k, (128 * c.pi + 64 * c.th + 16 * w) * 1280 + 160 * c.cs, 0);
    ep = ld64(c.rs, 0, OFF_CTRL);
    const int xv = x_voff(c, lane);
    SB();
    publish_x(c, 0, xv, (g & 1) ? OOR : xv, xc);
    unsigned xp[10], d0, d1;
#pragma unroll
    for (int k = 0; k < 2; ++k) { xp[4 * k] = xc[k].x; xp[4 * k + 1] = xc[k].y; xp[4 * k + 2] = xc[k].z; xp[4 * k + 3] = xc[k].w; }
    uswap16(xc[2].x, xc[2].z, xp[8], d0); uswap16(xc[2].y, xc[2].w, xp[9], d1);
    publish_stats(c, 0, lane, xp);
    c.ep = next_epoch(ep);
    int f0 = F_X + (((c.pi * 2 + c.th) * 4 + w) * 8 + c.cs) * 16; OPQ(f0);
    STAMP(101);
    DRAIN();
    set_flag(c, lane == 0 ? f0 : OOR);
    STAMP(102);
    __builtin_amdgcn_s_sleep(8);
    wait_flags(c, t16 * 16, F_X + (c.pi * 4 + w) * 256, 4);
    STAMP(103);
    int bq = c.oo + 1024, dq = c.oo + 1024 + 20480 - 256; OPQ(bq); OPQ(dq);
    poison128(c.rs, l16, c.oo); poison128(c.rs, l16, c.oo + 20480);
    poison128(c.rs, lane < 32 ? l16 + (lane < 16 ? bq : dq) : OOR, 0);
    poison128(c.rs, l16, c.ko + 512 + w * 1024);
    SB();
    poison_all<0>(c, LSZ, 7, l16, lane);
  } else {
    LAS char* S = L + L_Q + w * 12800;
    const int kind = w == 2 ? 2 : (w == 3 ? (c.pi < 8 ? c.pi >> 2 : 3) : w), kb = w == 3 ? 16 + (c.pi & 3) : c.pi;
    const rsrc_t rw = __builtin_amdgcn_make_buffer_rsrc((void*)(kind == 2 ? (c.th ? p.wo[1] : p.wo[0]) : (kind ? p.wqkv[1] : p.wqkv[0])), 0, kind == 3 ? 0 : (kind == 2 ? 640 * 1280 : 640 * 3840), 0x00020000);
    const int vq = kind == 2 ? (t16 < 10 ? g * 1280 + t16 * 16 : OOR) : (t16 < 15 ? g * 3840 + t16 * 16 + (t16 >= 5 ? 1200 : 0) + (t16 >= 10 ? 1200 : 0) : OOR);
    const int so = kind == 2 ? c.cs * 160 + 40 * kb * 1280 : c.pj * 80 + 32 * kb * 3840, st = kind == 2 ? 4 * 1280 : 4 * 3840;
    v2u cp[8];
    {
      v4u cw[10];
#pragma unroll
      for (int k = 0; k < 10; ++k) cw[k] = __builtin_amdgcn_raw_buffer_load_b128(rw, vq, (k < 8 || kind == 2) ? so + k * st : OOR, 0);
      ep = ld64(c.rs, 0, OFF_CTRL);
      if (kind == 1) {
        const rsrc_t rp = __builtin_amdgcn_make_buffer_rsrc((void*)p.wphi, 0, 32 * 800, 0x00020000);
        const int d = lane >> 3, q8 = lane & 7;
#pragma unroll
        for (int k = 0; k < 8; ++k) cp[k] = __builtin_amdgcn_raw_buffer_load_b64(rp, (d + 8 * (k & 3) < 20 && q8 < 5) ? d * 40 + q8 * 8 : OOR, (2 * c.pj + (k >> 2)) * 800 + (k & 3) * 320, 0);
      }
      SB();
#pragma unroll
      for (int k = 0; k < 10; ++k) *(LAS v4u*)(S + l16 + k * 1024) = cw[k];
    }
    c.ep = next_epoch(ep);
    if (w == 0 && lane < 24) *(LAS unsigned*)(L + L_ZERO + 4 * lane) = 0u;
    if (kind == 1) {
#pragma unroll
        for (int k = 0; k < 8; ++k) *(LAS v2u*)(S + 8192 + lane * 8 + k * 512) = cp[k];
        LAS char* pa = S + 8192 + 64 * (8 * g + q4) + 8 * p4;
        LAS char* pr = S + t16 * 256;
        LAS char* pb = pr + (g < 3 ? g * 16 : 0); LAS char* pb2 = pb + (g == 2 ? 0 : 8);
        f32x4 o0[8], o1[8];
#pragma unroll
        for (int u = 0; u < 8; ++u) {
          const bf16x8 f0 = tr2(pa + (u & 1) * 2048, 0, 256), f1 = tr2(pa + (u & 1) * 2048, 32, 288);
          const v2u lo = *(LAS v2u*)(pb + (u & 3) * 40 + (u >> 2) * 4096), hi = *(LAS v2u*)(pb2 + (u & 3) * 40 + (u >> 2) * 4096);
          const bf16x8 bb = asb((v4u){lo.x, lo.y, hi.x, hi.y});
          o0[u] = mfma16(f0, bb, (f32x4){}); o1[u] = mfma16(f1, bb, (f32x4){});
        }
        SB();
#pragma unroll
        for (int u = 0; u < 8; ++u) {
          *(LAS v2u*)(pr + 8 * g + (u & 3) * 40 + (u >> 2) * 4096) = (v2u){pk2(o0[u][0], o0[u][1]), pk2(o0[u][2], o0[u][3])};
          *(LAS v2u*)(g == 0 ? pr + 32 + (u & 3) * 40 + (u >> 2) * 4096 : S + 12288) = (v2u){pk2(o1[u][0], o1[u][1]), pk2(o1[u][2], o1[u][3])};
        }
        SB();
    }
    v4u f[8];
    if (kind != 2) {
      LAS char* pt = S + (8 * g + q4) * 256 + (p4 >> 1) * 40;
      const int hd = p4 >> 1;
#pragma unroll
      for (int T = 0; T < 8; ++T) {
        const int sq = T < 5 ? T : ((p4 & 1) ? (T < 7 ? T - 2 : 2) : T - 5);
        const int qd = sq + hd >= 5 ? sq + hd - 5 : sq + hd;
        f[T] = __builtin_bit_cast(v4u, tr2(pt + (T < 5 ? 80 + 80 * (p4 & 1) : 0) + 8 * qd, 0, 1024));
      }
    } else {
#pragma unroll
      for (int k = 0; k < 8; ++k) {
        const int gc = min(4 * k + g, 24), rg = (gc * 13) >> 6, ct = gc - rg * 5;
        f[k] = __builtin_bit_cast(v4u, tr2(S + (8 * rg + q4) * 256 + 32 * ct + 8 * p4, 0, 1024));
      }
    }
    SB();
    v4u im[25];
    volatile LAS unsigned* go = (volatile LAS unsigned*)(L + L_GO + 8);
    NOUNROLL for (int pass = 0; pass < 2; ++pass) {
      if (pass == 1) {
        if (w == 0) {
          __builtin_amdgcn_s_sleep(PW1);
          wait_flags(c, min(lane, 19) * 32, F_A + c.pj * 1024, 6);
          go[0] = c.ep.x; go[1] = ~c.ep.y;
        } else {
          NOUNROLL for (unsigned spins = 0; spins < (SPIN_CAP << 4); ++spins) { if (go[0] == c.ep.x && go[1] == ~c.ep.y) break; __builtin_amdgcn_s_sleep(1); }
          asm volatile("" ::: "memory");
        }
        STAMP(106);
#pragma unroll
        for (int k = 0; k < 25; ++k) im[k] = ldi(c.rs, l16, OFF_WT + c.pj * WT_SZ + w * 25600, k * 1024);
        SB();
      }
      if (pass == kind) {
        const int tv7 = (t16 & 4) ? OOR : g * 128 + (t16 >> 3) * 64 + (t16 & 3) * 16, ib = OFF_WT + (16 * kind + c.pj) * WT_SZ;
#pragma unroll
        for (int T = 0; T < 8; ++T) st128r(c.rs, T < 7 ? l16 : tv7, ib + (T < 5 ? (kb * 5 + T) * 1024 : L_Q + kb * 2560 + (T - 5) * 1024), f[T]);
        if (kind == 0) {
          STAMP(104);
          DRAIN();
          int f0 = F_A + (c.pj * 32 + kb) * 32; OPQ(f0);
          set_flag(c, lane == 0 ? f0 : OOR);
          STAMP(105);
        }
        float sm[8];
#pragma unroll
        for (int T = 0; T < 8; ++T) sm[T] = xsum32(xsum16(sum2(f[T].x, sum2(f[T].y, sum2(f[T].z, sum2(f[T].w, 0.f))))));
        st128r(c.rs, g < 2 ? t16 * 32 + g * 16 : OOR, OFF_CS + (kind * 16 + c.pj) * 10240 + kb * 512, g ? (v4u){f2u(sm[4]), f2u(sm[5]), f2u(sm[6]), f2u(sm[7])} : (v4u){f2u(sm[0]), f2u(sm[1]), f2u(sm[2]), f2u(sm[3])});
        if (kind == 0) {
          DRAIN();
          int f0 = F_A + (c.pj * 32 + kb) * 32 + 16; OPQ(f0);
          set_flag(c, lane == 0 ? f0 : OOR);
        }
      }
    }
    if (kind == 2) {
#pragma unroll
      for (int k = 0; k < 7; ++k) {
        const int gi = 4 * k + g, rg = (gi * 13) >> 6, ct = gi - rg * 5, RG = 5 * c.pi + rg;
        st128r(c.rs, gi < 25 ? (((RG >> 2) * 5 + ct) * 64 + (RG & 3) * 16 + t16) * 16 : OOR, OFF_WO + c.pj * WO_SZ, f[k]);
      }
    }
    SB();
#pragma unroll
    for (int k = 0; k < 25; ++k) *(LAS v4u*)(L + w * 25600 + l16 + k * 1024) = im[k];
    if (w == 1 && lane < 2) *(LAS unsigned*)(L + L_GO + 4 * lane) = 0u;
    STAMP(108);
  }
  asm volatile("" :: "v"(pf));
  int role = w8; OPQ(role);
  if (role < 4) {
    for (int it = 0; it < 2; ++it) {
      layer_c<false>(c, p, 2 * it);
      layer_c<true>(c, p, 2 * it + 1);
    }
    if (blockIdx.x == 0 && w == 0) set_flag(c, lane_id() == 0 ? 0 : OOR);
    STAMP(100); CSTAMP(121);
  } else {
    __builtin_amdgcn_s_setprio(HPRIO);
    for (int it = 0; it < 2; ++it) {
      layer_h<false>(c, 2 * it);
      layer_h<true>(c, 2 * it + 1);
    }
  }
  DUMP();
}

extern "C" void kernel_launch(void* const* d_in, const int* in_sizes, int n_in, void* d_out, int out_size, void* d_ws, size_t ws_size, hipStream_t stream) {
  Params p;
  p.x = (const unsigned short*)d_in[0];
  p.wqkv[0] = (const unsigned short*)d_in[1]; p.wo[0] = (const unsigned short*)d_in[2];
  p.wqkv[1] = (const unsigned short*)d_in[4]; p.wphi = (const unsigned short*)d_in[5]; p.wo[1] = (const unsigned short*)d_in[6];
  p.ws = (unsigned char*)d_ws; p.out = (unsigned short*)d_out;
  (void)hipFuncSetAttribute((const void*)mega, hipFuncAttributeMaxDynamicSharedMemorySize, LDS_BYTES);
  mega<<<256, 512, LDS_BYTES, stream>>>(p);
}
```

```cpp
#define PLATE 1
#include <hip/hip_runtime.h>
#include <hip/hip_bf16.h>
#include <stdint.h>

typedef unsigned v4u __attribute__((ext_vector_type(4)));
typedef unsigned v2u __attribute__((ext_vector_type(2)));
typedef short bf16x8 __attribute__((ext_vector_type(8)));
typedef short s16x4 __attribute__((ext_vector_type(4)));
typedef float f32x2 __attribute__((ext_vector_type(2)));
typedef float f32x4 __attribute__((ext_vector_type(4)));
typedef float f32x16 __attribute__((ext_vector_type(16)));
typedef __bf16 bf16x2v __attribute__((ext_vector_type(2)));
#define LAS __attribute__((address_space(3)))
#define GAS __attribute__((address_space(1)))
#define DI __device__ __forceinline__
#define AI __attribute__((always_inline))
typedef __amdgpu_buffer_rsrc_t rsrc_t;

constexpr int SC1 = 16;
constexpr unsigned POISON = 0xFFFFFFFFu;
constexpr unsigned SPIN_CAP = 1u << 16;
constexpr int OOR = 0x7FFF0000;

constexpr int XB_SZ = 2048 * 640 * 2;
constexpr int KVSLOT = 8192;
constexpr int R_X = 0, R_O = XB_SZ, R_KV = 2 * XB_SZ;
constexpr int LSZ = 2 * XB_SZ + 256 * KVSLOT;
constexpr int WT_SZ = 153600, WO_SZ = 102400;
constexpr int OFF_WT = 4 * LSZ;
constexpr int OFF_CS = OFF_WT + 2 * 16 * WT_SZ;
constexpr int OFF_WO = OFF_CS + 32 * 10240;
constexpr int OFF_HB = OFF_WO + 2 * 8 * WO_SZ;
constexpr int OFF_CTRL = OFF_HB + 32768;
constexpr int F_X = 4096, F_R = F_X + 16384, F_H = F_R + 16384, F_A = F_H + 16384;
constexpr int OFF_DBG = OFF_CTRL + F_A + 16384;
constexpr int WS_USED = OFF_DBG + (1 << 20);

constexpr int L_Q = 102400, L_MISC = 153600;
constexpr int L_KV = L_MISC;
constexpr int L_ZERO = L_KV + 4032;
constexpr int L_DUMP = L_ZERO + 96;
constexpr int L_CS = L_DUMP + 16;
constexpr int L_ST = L_CS + 2048;
constexpr int L_GO = L_ST + 1024;
constexpr int LDS_BYTES = 163840;

#define OPQ(x) do { x = __builtin_amdgcn_readfirstlane(x); asm volatile("" : "+s"(x)); } while (0)
#define OPQV(x) asm volatile("" : "+v"(x))
#define PINA(x) OPQV(x)
#define SB() __builtin_amdgcn_sched_barrier(0)
#define DRAIN() asm volatile("s_waitcnt vmcnt(0)" ::: "memory")
#define VMWAIT(n) asm volatile("s_waitcnt vmcnt(%0)" :: "i"(n) : "memory")
#define NOUNROLL _Pragma("nounroll")
DI v4u ld128(rsrc_t rs, int voff, int soff) { return __builtin_amdgcn_raw_buffer_load_b128(rs, voff, soff, SC1); }
DI v2u ld64(rsrc_t rs, int voff, int soff) { return __builtin_amdgcn_raw_buffer_load_b64(rs, voff, soff, SC1); }
DI v4u ldi(rsrc_t rs, int& voff, int soff, int coff) { OPQV(voff); return ld128(rs, voff + (coff & 4095), soff + (coff & ~4095)); }
DI unsigned np(unsigned u) { return u < 0xFFFFFFFEu ? u : 0xFFFFFFFEu; }
DI void st128r(rsrc_t rs, int voff, int soff, v4u v) { __builtin_amdgcn_raw_buffer_store_b128(v, rs, voff, soff, SC1); asm volatile("s_nop 1" :: "v"(v)); }
DI void st128(rsrc_t rs, int voff, int soff, v4u v) { st128r(rs, voff, soff, (v4u){v.x, np(v.y), v.z, np(v.w)}); }
DI void st64(rsrc_t rs, int voff, int soff, v2u v) { v2u q = {v.x, np(v.y)}; __builtin_amdgcn_raw_buffer_store_b64(q, rs, voff, soff, SC1); }
DI void stp128(rsrc_t rs, int voff, int soff, v4u v) { __builtin_amdgcn_raw_buffer_store_b128(v, rs, voff, soff, 0); asm volatile("s_nop 1" :: "v"(v)); }
DI void poison128(rsrc_t rs, int voff, int soff) { st128r(rs, voff, soff, (v4u){POISON, POISON, POISON, POISON}); }
DI unsigned pk2(float a, float b) { f32x2 v = {a, b}; return __builtin_bit_cast(unsigned, __builtin_convertvector(v, bf16x2v)); }
DI float u2f(unsigned u) { return __builtin_bit_cast(float, u); }
DI unsigned f2u(float f) { return __builtin_bit_cast(unsigned, f); }
DI float rbf(float a) { return u2f(pk2(a, a) & 0xFFFF0000u); }
DI float blo(unsigned u) { return u2f(u << 16); }
DI float bhi(unsigned u) { return u2f(u & 0xFFFF0000u); }
DI float sum2(unsigned u, float acc) { return __builtin_amdgcn_fdot2_f32_bf16(__builtin_bit_cast(bf16x2v, u), __builtin_bit_cast(bf16x2v, 0x3F803F80u), acc, false); }
DI unsigned umax3(unsigned a, unsigned b, unsigned c) { return max(max(a, b), c); }
DI unsigned mx4(unsigned m, v4u v) { return umax3(m, v.y, v.w); }
DI bf16x8 tr2(LAS char* p, int off1, int off2) {
  s16x4 a = __builtin_amdgcn_ds_read_tr16_b64_v4i16((LAS s16x4*)(p + off1));
  s16x4 b = __builtin_amdgcn_ds_read_tr16_b64_v4i16((LAS s16x4*)(p + off2));
  return __builtin_shufflevector(a, b, 0, 1, 2, 3, 4, 5, 6, 7);
}
DI bf16x8 rd(LAS char* p, int off) { return *(LAS bf16x8*)(p + off); }
DI f32x16 mfma32(bf16x8 a, bf16x8 b, f32x16 c) { return __builtin_amdgcn_mfma_f32_32x32x16_bf16(a, b, c, 0, 0, 0); }
DI f32x4 mfma16(bf16x8 a, bf16x8 b, f32x4 c) { return __builtin_amdgcn_mfma_f32_16x16x32_bf16(a, b, c, 0, 0, 0); }
DI bf16x8 asb(v4u v) { return __builtin_bit_cast(bf16x8, v); }
DI float xsum32(float v) { unsigned u = f2u(v); auto r = __builtin_amdgcn_permlane32_swap(u, u, false, false); return u2f(r[0]) + u2f(r[1]); }
DI float xsum16(float v) { unsigned u = f2u(v); auto r = __builtin_amdgcn_permlane16_swap(u, u, false, false); return u2f(r[0]) + u2f(r[1]); }
DI int lane_id() { int l; asm volatile("v_mbcnt_lo_u32_b32 %0, -1, 0\n\tv_mbcnt_hi_u32_b32 %0, -1, %0" : "=v"(l)); return l; }
DI unsigned vzero() { unsigned z; asm volatile("v_mov_b32 %0, 0" : "=v"(z)); return z; }
DI unsigned vone() { unsigned z; asm volatile("v_mov_b32 %0, 0x3f80" : "=v"(z)); return z; }
DI void bar() { asm volatile("s_waitcnt lgkmcnt(0)" ::: "memory"); __builtin_amdgcn_s_barrier(); asm volatile("" ::: "memory"); }
#define MF16(acc, a, b) asm volatile("v_mfma_f32_16x16x32_bf16 %0, %1, %2, %0" : "+v"(acc) : "v"(a), "v"(b))
#define MFIN() asm volatile("s_nop 7\n\ts_nop 1" ::: "memory")
#define MF32(acc, a, b) do { acc = mfma32(a, b, acc); PINA(acc); SB(); } while (0)
DI void stw(LAS char* p, v4u& v) { *(LAS v4u*)p = v; }
#define RAWBAR() do { SB(); __builtin_amdgcn_s_barrier(); SB(); } while (0)
template <int I> struct IC { static constexpr int v = I; };
template <int B, int E, class F> DI void sfor(F&& f) { if constexpr (B < E) { f(IC<B>{}); sfor<B + 1, E>(f); } }

#define STAMP(k) do {} while (0)
#define CL 2
#define CSTAMP(k) do {} while (0)
#define DUMP()
#define ZST(i)
#define RCOUNT(k)
#define LSTAMP(k) do { STAMP(1 + lyr * 24 + (k)); if ((k) >= 2 && (k) < 8 && lyr == CL) CSTAMP(112 + (k)); } while (0)

struct Params {
  const unsigned short* x; const unsigned short* wqkv[2]; const unsigned short* wo[2]; const unsigned short* wphi;
  unsigned char* ws; unsigned short* out;
};
struct Ctx { rsrc_t rs; LAS char* L; int pi, pj, th, cs, w, lane; int xo, oo, ko; v2u ep; };

template <int N> DI unsigned chk128(const v4u* x) { unsigned m = 0;
#pragma unroll
  for (int k = 0; k < N; ++k) m = mx4(m, x[k]);
  return m; }
template <int N, int G0, int G1>
DI void fixup(const Ctx& c, v4u* x, unsigned mk, int voff, int soff, int stride, int kind) {
  if (__builtin_expect(__any(mk == POISON), 0)) {
    NOUNROLL for (unsigned spins = 0; spins < SPIN_CAP; ++spins) {
      OPQ(soff);
      bool bad[G1 - G0];
#pragma unroll
      for (int q = G0; q < G1; ++q) bad[q - G0] = __any(chk128<N>(x + N * q) == POISON);
      SB();
#pragma unroll
      for (int q = G0; q < G1; ++q) {
        if (bad[q - G0]) {
#pragma unroll
          for (int k = N * q; k < N * q + N; ++k) x[k] = ld128(c.rs, voff, soff + k * stride);
        }
      }
      if (!bad[0]) break;
      RCOUNT(kind);
    }
    __builtin_amdgcn_s_waitcnt(0x0F70);
  }
}

template <bool ISB>
DI void phi20(const float* v, float rs, unsigned* out) {
  const float rl = rs * 1.44269504088896f;
  if constexpr (!ISB) {
    float r[20];
#pragma unroll
    for (int i = 0; i < 20; ++i) {
      const float y = v[i] * rl;
      r[i] = fmaf(__builtin_amdgcn_fmed3f(y, 1.44269504e-6f, __builtin_inff()), 0.693147180559945f, __builtin_amdgcn_fmed3f(__builtin_amdgcn_exp2f(y), 0.f, 1.f));
    }
#pragma unroll
    for (int i = 0; i < 10; ++i) out[i] = pk2(r[2 * i], r[2 * i + 1]);
  } else {
    float mx = __builtin_amdgcn_fmed3f(v[0], v[1], __builtin_inff()), mn = __builtin_amdgcn_fmed3f(v[0], v[1], -__builtin_inff());
#pragma unroll
    for (int i = 2; i < 20; i += 2) { mx = fmaxf(fmaxf(mx, v[i]), v[i + 1]); mn = fminf(fminf(mn, v[i]), v[i + 1]); }
    const float cp = -mx * rl, cn = mn * rl;
    const f32x2 rl2 = {rl, rl}, cp2 = {cp, cp}, cn2 = {cn, cn};
    f32x2 ep[10], en[10], sp, sn;
#pragma unroll
    for (int i = 0; i < 10; ++i) {
      const f32x2 a = {v[2 * i], v[2 * i + 1]};
      const f32x2 yp = __builtin_elementwise_fma(a, rl2, cp2), yn = __builtin_elementwise_fma(a, -rl2, cn2);
      ep[i] = (f32x2){__builtin_amdgcn_exp2f(yp[0]), __builtin_amdgcn_exp2f(yp[1])};
      en[i] = (f32x2){__builtin_amdgcn_exp2f(yn[0]), __builtin_amdgcn_exp2f(yn[1])};
      sp = i ? sp + ep[i] : ep[i]; sn = i ? sn + en[i] : en[i];
    }
    const float ip = __builtin_amdgcn_rcpf(sp[0] + sp[1]), in = __builtin_amdgcn_rcpf(sn[0] + sn[1]);
#pragma unroll
    for (int i = 0; i < 10; ++i) { const f32x2 a = ep[i] * ip, b = en[i] * in; out[i] = pk2(a[0], a[1]); out[10 + i] = pk2(b[0], b[1]); }
  }
}
DI void swap16(float a, float b, float& lo, float& hi) { auto r = __builtin_amdgcn_permlane16_swap(f2u(a), f2u(b), false, false); lo = u2f(r[0]); hi = u2f(r[1]); }

template <int SL>
DI void poison_all(const Ctx& c, int lb, int n, int l16, int lane) {
  const int odd = c.cs & 1, s0 = (5 * c.cs + odd) >> 1;
  int xf = c.xo + (s0 * 2 + (c.w & 1)) * 1024, a = c.xo + (odd ? s0 - 1 : s0 + 2) * 2048 + odd * 512, b = c.oo + 1024 - 512, d = c.oo + 1024 + 20480 - 768;
  OPQ(xf); OPQ(a); OPQ(b); OPQ(d);
#define PZ() do { if (SL) __builtin_amdgcn_s_sleep(SL); } while (0)
  poison128(c.rs, l16, lb + xf); PZ(); poison128(c.rs, l16 + 1024, lb + xf); PZ();
  poison128(c.rs, l16, lb + c.oo); PZ(); poison128(c.rs, l16, lb + c.oo + 20480); PZ();
  poison128(c.rs, l16 + (lane < 32 ? a : (lane < 48 ? b : d)), lb); PZ();
  poison128(c.rs, l16, lb + c.ko + c.w * 1024); PZ();
  if (n > 6) poison128(c.rs, l16, lb + c.ko + c.w * 1024 + 4096);
}
DI int x_piece(const Ctx& c, int ct) { const int ch = 10 * c.cs + 2 * ct; return c.xo + (ch >> 2) * 2048 + (ch & 3) * 256; }
DI void uswap16(unsigned a, unsigned b, unsigned& lo, unsigned& hi) { auto r = __builtin_amdgcn_permlane16_swap(a, b, false, false); lo = r[0]; hi = r[1]; }
DI void x_chunks(const unsigned* xp, v4u* ch) {
#pragma unroll
  for (int k = 0; k < 3; ++k) {
    unsigned a0, a1, b0, b1;
    uswap16(xp[4 * k], xp[k < 2 ? 4 * k + 2 : 8], a0, b0); uswap16(xp[4 * k + 1], xp[k < 2 ? 4 * k + 3 : 9], a1, b1);
    ch[k] = (v4u){a0, a1, b0, b1};
  }
}
DI int x_voff(const Ctx& c, int lane) { int d = (c.cs & 1) ? 1536 : 512; OPQ(d); return ((lane >> 4) & 1) * d + (lane >> 5) * 256 + (lane & 15) * 16; }
DI void publish_x(const Ctx& c, int lb, int xv, int xv4, const v4u* ch) {
#pragma unroll
  for (int k = 0; k < 3; ++k) st128(c.rs, k < 2 ? xv : xv4, lb + x_piece(c, 2 * k), ch[k]);
}
DI void publish_stats(const Ctx& c, int lb, int lane, const unsigned* xp) {
  float s = 0.f;
#pragma unroll
  for (int i = 0; i < 10; ++i) s = sum2(xp[i], s);
  s = xsum32(xsum16(s));
  const float m = s * (1.f / 80.f);
  f32x2 q2 = {0.f, 0.f};
#pragma unroll
  for (int i = 0; i < 10; ++i) { const f32x2 d = (f32x2){blo(xp[i]), bhi(xp[i])} - m; q2 = __builtin_elementwise_fma(d, d, q2); }
  const float q = xsum32(xsum16(q2[0] + q2[1]));
  st64(c.rs, lane < 16 ? lane * 8 : OOR, lb + c.ko + c.w * 128, (v2u){f2u(s), f2u(q)});
}
#define HINT 0
#define PD3 0
#define PS 1
#define OPF 10
#define PD1 3
#define PD2 3
DI v2u next_epoch(v2u e) { unsigned x = __builtin_amdgcn_readfirstlane(e.x) + 1u, y = __builtin_amdgcn_readfirstlane(e.y) + (x == 0u); return (v2u){x, y}; }
DI void wait_flags(const Ctx& c, int voff, int soff, int kind) {
  NOUNROLL for (unsigned spins = 0; spins < SPIN_CAP; ++spins) {
    const v2u f = ld64(c.rs, voff, OFF_CTRL + soff);
    if (__all(f.x == c.ep.x && f.y == c.ep.y)) break;
    RCOUNT(kind);
    __builtin_amdgcn_s_sleep(2);
  }
  asm volatile("" ::: "memory");
}
DI void set_flag(const Ctx& c, int voff) { __builtin_amdgcn_raw_buffer_store_b64(c.ep, c.rs, voff, OFF_CTRL, SC1); }
DI void row_stats(const Ctx& c, int lb, int lane) {
  const int w = c.w, t32 = lane & 31, hh = lane >> 5;
  v2u stv[4];
  const int sv = hh * (4 * 16 * KVSLOT) + t32 * 8; int ss = lb + R_KV + ((w >> 1) * 128 + c.pi) * KVSLOT + (w & 1) * 256;
  NOUNROLL for (unsigned spins = 0; spins < SPIN_CAP; ++spins) {
#pragma unroll
    for (int q = 0; q < 4; ++q) stv[q] = ld64(c.rs, sv, ss + q * 16 * KVSLOT);
    if (!__any(max(umax3(stv[0].y, stv[1].y, stv[2].y), stv[3].y) == POISON)) break;
    RCOUNT(3);
    __builtin_amdgcn_s_sleep(4);
  }
  const float mean = xsum32((u2f(stv[0].x) + u2f(stv[1].x)) + (u2f(stv[2].x) + u2f(stv[3].x))) * (1.f / 640.f);
  float m2 = 0.f;
#pragma unroll
  for (int q = 0; q < 4; ++q) { const float d = u2f(stv[q].x) * (1.f / 80.f) - mean; m2 += fmaf(80.f * d, d, u2f(stv[q].y)); }
  const float var = rbf(xsum32(m2) * (1.f / 640.f));
  const float rs = __builtin_amdgcn_rcpf(rbf(__builtin_amdgcn_sqrtf(rbf(var + 1e-5f))));
  *(LAS v2u*)(c.L + L_ST + w * 256 + t32 * 8) = (v2u){pk2(-mean, -mean), f2u(rs)};
}
DI void colsum(const Ctx& c, int t, int lane) {
  const int i = (c.w & 1) * 64 + lane;
  unsigned cs[20];
#pragma unroll
  for (int k = 0; k < 20; ++k) cs[k] = __builtin_amdgcn_raw_buffer_load_b32(c.rs, i * 4 + (k & 7) * 512, OFF_CS + (t * 16 + c.pj) * 10240 + (k >> 3) * 4096, SC1);
  float s = 0.f;
#pragma unroll
  for (int k = 0; k < 20; ++k) s += u2f(cs[k]);
  const float hi = rbf(s), mid = rbf(s - hi), lo = rbf(s - hi - mid);
  *(LAS v2u*)(c.L + L_CS + t * 1024 + i * 8) = (v2u){pk2(hi, mid), pk2(lo, 0.f)};
}
#define PW0 12
#define PW1 0
#define HPRIO 0
#define HVE 0
#define PPOS 0
#define PSL 0
#define STAG 0
#define OAUX 0
#define XRA 10
#define XRN 12
#define XE 4
#define PAN 8
#define XPN 16
#define RQ 3
#define B4N 69
template <bool ISB>
DI void layer_c(const Ctx& c, const Params& p, const int lyr) {
  unsigned xp[10]; int xrv, xr4; v4u xc[3];
  int lane = lane_id();
  const int w = c.w, t32 = lane & 31, hh = lane >> 5, t16 = lane & 15, g = lane >> 4;
  constexpr int NQ = ISB ? 10 : 5, NKS = ISB ? 5 : 3, TRS = ISB ? 160 : 96;
  constexpr int type = ISB ? 1 : 0;
  LAS char* L = c.L;
  int l16 = lane * 16;
  LAS char* wa = L + l16;
  const int lb = __builtin_amdgcn_readfirstlane(lyr * LSZ);
  v4u xf[40];
  const int xs = lb + R_X + (c.pi * 4 + w) * 40960;
  const int q4 = (lane & 15) >> 2, p4 = lane & 3;
  LAS char* wh = wa + 51200;
  constexpr int VO = ISB ? 80 : 48;
  LAS char* dst = L + hh * 20480 + (32 * w + t32) * TRS;
  LAS char* ta = L + (w & 1) * 20480 + (4 * g + q4) * TRS + 8 * p4;
  LAS char* tb = ta + VO + 32 * (w >> 1) - (((w >> 1) && p4 >= 2) ? 16 : 0);
  int kvv = 16 * (w >> 1) + t16 < 21 ? g * 336 + (16 * (w >> 1) + t16) * 16 : OOR;
  int kvw = g < NQ % 4 ? kvv : OOR;
  LAS char* csa = L + (g ? L_ZERO : L_CS + type * 1024 + t16 * 64);
  LAS char* wq = L + L_ST + w * 256 + t16 * 8;
  LAS char* rsa = L + L_ST + w * 256 + t32 * 8 + 4;
  OPQV(wh); OPQV(dst); OPQV(ta); OPQV(tb); OPQV(kvv); OPQV(kvw); OPQV(csa); OPQV(wq); OPQV(rsa);
  SB();
  if (ISB || lyr) { VMWAIT(1); __builtin_amdgcn_s_sleep(PD1); }
  LSTAMP(1);
  constexpr int XP = XPN, XR = ISB ? XRN : XRA;
#pragma unroll
  for (int k = 0; k < XP; ++k) { xf[k] = ldi(c.rs, l16, xs, k * 1024); SB(); }
  if (!ISB) VMWAIT(XP);
  RAWBAR();
  LSTAMP(2);
  f32x4 a[5][2] = {};
  constexpr int PA = PAN;
  bf16x8 A[PA];
#pragma unroll
  for (int t = 0; t < 5; ++t) { OPQV(a[t][0]); OPQV(a[t][1]); }
  SB();
#pragma unroll
  for (int n = 0; n < PA; ++n) A[n] = rd(wa, n * 1024);
  SB();
  float rs = 0.f; v4u cs[4], mlo[2], mhi[2]; unsigned mm[2];
  LAS char* wq2;
  unsigned mk = chk128<4>(xf);
  sfor<0, 100>([&](auto N) AI {
    constexpr int n = decltype(N)::v, s = n / 5, t = n % 5, gq = n / 10, u = n % 10;
    if constexpr (u == 0) { fixup<4, gq, (XP / 4 + gq < 10 ? XP / 4 + gq : 10)>(c, xf, mk, l16, xs, 1024, 0); SB(); }
    if constexpr ((n & 1) == 0) { OPQV(A[(n + 1) % PA]); SB(); }
    MF16(a[t][0], A[n % PA], asb(xf[2 * s])); SB();
    if constexpr ((u == 0 || u == 2 || u == 4 || u == 8) && XP + 4 * gq + (u == 8 ? 3 : u / 2) < 40) { constexpr int k = XP + 4 * gq + (u == 8 ? 3 : u / 2); xf[k] = ldi(c.rs, l16, xs, k * 1024); }
    if constexpr (gq < 9 && (u == 5 || u == 7)) { mk = mx4(u == 5 ? 0u : mk, xf[4 * gq + u]); OPQV(mk); }
    if constexpr (n == 70) { mm[0] = *(LAS unsigned*)(wq); mm[1] = *(LAS unsigned*)(wq + 128); }
    if constexpr (n == 71) { rs = *(LAS float*)(rsa); }
    if constexpr (n == 72) { cs[0] = *(LAS v4u*)(csa); }
    if constexpr (n == 73) { cs[1] = *(LAS v4u*)(csa + 16); }
    if constexpr (n == 74) { cs[2] = *(LAS v4u*)(csa + 32); }
    if constexpr (n == 84 || n == 85) { mlo[n - 84].z = vzero(); mlo[n - 84].w = vzero(); }
    if constexpr (n == 86 || n == 87) { mhi[n - 86].x = vzero(); mhi[n - 86].y = vzero(); }
    if constexpr (n == 88) { OPQV(mlo[0]); OPQV(mlo[1]); OPQV(mhi[0]); OPQV(mhi[1]); }
    if constexpr (n == 80 || n == 81) { mlo[n - 80].x = mm[n - 80]; mlo[n - 80].y = mm[n - 80]; }
    if constexpr (n == 82 || n == 83) { mhi[n - 82].z = mm[n - 82]; mhi[n - 82].w = mm[n - 82]; }
    SB();
    MF16(a[t][1], A[n % PA], asb(xf[2 * s + 1])); SB();
    if constexpr (n + PA < 100) A[n % PA] = (n + PA < 50) ? rd(wa, (n + PA) * 1024) : rd(wh, (n + PA - 50) * 1024);
    if constexpr (gq < 9 && (u == 6 || u == 8)) { mk = mx4(mk, xf[4 * gq + u - 2]); OPQV(mk); }
    SB();
    if constexpr (n >= 90 && n < 95) {
      constexpr int q = (n + 2) % 5;
      MF16(a[q][0], asb(cs[q >> 1]), asb((q & 1) ? mhi[0] : mlo[0])); SB();
      MF16(a[q][1], asb(cs[q >> 1]), asb((q & 1) ? mhi[1] : mlo[1])); SB();
    }
    if constexpr (n == B4N) RAWBAR();
  });
  MFIN();
#pragma unroll
  for (int t = 0; t < 5; ++t) { OPQV(a[t][0]); OPQV(a[t][1]); }
  SB();
  LSTAMP(3);
  {
    float kx[20], vx[20];
#pragma unroll
    for (int i = 0; i < 20; ++i) swap16(a[i >> 2][0][i & 3], a[i >> 2][1][i & 3], kx[i], vx[i]);
    unsigned v[10], o[ISB ? 20 : 10];
#pragma unroll
    for (int i = 0; i < 10; ++i) v[i] = pk2(vx[2 * i] * rs, vx[2 * i + 1] * rs);
    *(LAS v4u*)(dst + VO) = (v4u){v[0], v[1], v[2], v[3]}; *(LAS v4u*)(dst + VO + 16) = (v4u){v[4], v[5], v[6], v[7]};
    *(LAS v4u*)(dst + VO + 32) = (v4u){v[8], v[9], vone(), vzero()};
    phi20<ISB>(kx, rs, o);
#pragma unroll
    for (int i = 0; i < (ISB ? 5 : 2); ++i) *(LAS v4u*)(dst + 16 * i) = (v4u){o[4 * i], o[4 * i + 1], o[4 * i + 2], o[4 * i + 3]};
    if (!ISB) *(LAS v2u*)(dst + 32) = (v2u){o[8], o[9]};
  }
  SB();
  bar();
  LSTAMP(4);
  {
    constexpr int NF = ISB ? 3 : 2;
    bf16x8 fa[4][NF], fb[4];
    f32x4 kv[NF] = {};
#pragma unroll
    for (int ks = 0; ks < 6; ++ks) {
      if (ks < 4) {
        fb[ks] = tr2(tb, 32 * ks * TRS, (32 * ks + 16) * TRS);
#pragma unroll
        for (int ft = 0; ft < NF; ++ft) fa[ks][ft] = tr2(ta, 32 * ks * TRS + 32 * ft, (32 * ks + 16) * TRS + 32 * ft);
        SB();
      }
      if (ks >= 1 && ks - 1 < XE && ks - 1 < XR) { xf[ks - 1] = ldi(c.rs, l16, xs, (ks - 1) * 1024); SB(); }
      if (ks >= 2) {
#pragma unroll
        for (int ft = NF - 1; ft >= 0; --ft) { kv[ft] = mfma16(fa[ks - 2][ft], fb[ks - 2], kv[ft]); OPQV(kv[ft]); }
        SB();
      }
    }
    const int kvs = lb + c.ko + 512 + (w & 1) * NQ * 336;
    wq = wa + L_Q; wq2 = L + L_Q + 2048 + g * 128 + (t16 >> 3) * 64 + (t16 & 3) * 16;
    xrv = x_voff(c, lane); xr4 = (g & 1) ? OOR : xrv;
    OPQV(wq); OPQV(wq2); OPQV(xrv); OPQV(xr4);
    SB();
#pragma unroll
    for (int ft = NF - 1; ft >= 0; --ft) st128(c.rs, (ft < NF - 1 ? kvv : kvw) + ft * 1344, kvs, __builtin_bit_cast(v4u, kv[ft]));
  }
  RAWBAR();
  LSTAMP(5);
  int k0, k1, ov, xov;
  unsigned pq[NKS * 4];
  {
    f32x4 q[3][2] = {};
#pragma unroll
    for (int t = 0; t < 3; ++t) { OPQV(q[t][0]); OPQV(q[t][1]); }
    SB();
    auto rdq = [&](int n) AI { const int s = (n / 3 + XR / 2) % 20, t = n % 3; return t < 2 ? rd(wq, s * 2560 + t * 1024) : rd(wq2, s * 2560); };
#pragma unroll
    for (int n = 0; n < PA; ++n) A[n] = rdq(n);
    cs[2] = *(LAS v4u*)(csa + 32); cs[3] = *(LAS v4u*)(csa + 48);
    OPQV(mm[0]); OPQV(mm[1]);
#pragma unroll
    for (int h = 0; h < 2; ++h) { mlo[h] = (v4u){mm[h], mm[h], vzero(), vzero()}; mhi[h] = (v4u){vzero(), vzero(), mm[h], mm[h]}; OPQV(mlo[h]); OPQV(mhi[h]); }
    SB();
    sfor<0, 60>([&](auto N) AI {
      constexpr int n = decltype(N)::v, s = (n / 3 + XR / 2) % 20, t = n % 3;
      if constexpr ((n & 1) == 0) { OPQV(A[(n + 1) % PA]); SB(); }
      MF16(q[t][0], A[n % PA], asb(xf[2 * s])); SB();
      if constexpr (n % RQ == 0 && XE + n / RQ < XR) xf[XE + n / RQ] = ldi(c.rs, l16, xs, (XE + n / RQ) * 1024);
      if constexpr (n == 41 || n == 44 || n == 47) { constexpr int k = (n - 41) / 3; xc[k] = ld128(c.rs, k < 2 ? xrv : xr4, lb + x_piece(c, 2 * k)); }
      SB();
      MF16(q[t][1], A[n % PA], asb(xf[2 * s + 1])); SB();
      if constexpr (n + PA < 60) A[n % PA] = rdq(n + PA);
      SB();
      if constexpr (n >= 6 && n < 9) {
        constexpr int z = (n + 1) % 3;
        MF16(q[z][0], asb(cs[(5 + z) >> 1]), asb((z & 1) ? mlo[0] : mhi[0]));
        MF16(q[z][1], asb(cs[(5 + z) >> 1]), asb((z & 1) ? mlo[1] : mhi[1]));
        SB();
      }
    });
    MFIN();
#pragma unroll
    for (int t = 0; t < 3; ++t) { OPQV(q[t][0]); OPQV(q[t][1]); }
    SB();
    LSTAMP(6);
    {
      const int e0 = 4 * (t32 >> 3) + (t32 & 3), e1 = min(16 + e0, 20);
      const bool own = ((t32 >> 2) & 1) == hh;
      k0 = own ? L_KV + hh * 2016 + e0 * 96 : L_ZERO; k1 = own ? L_KV + hh * 2016 + e1 * 96 : L_ZERO;
      xov = (t32 >> 4) * 20480 + t16 * 16; ov = xov + 768 * hh; xov = hh ? OOR : xov;
      OPQV(k0); OPQV(k1); OPQV(ov); OPQV(xov);
    }
    float qx[24];
#pragma unroll
    for (int i = 0; i < 12; ++i) swap16(q[i >> 2][0][i & 3], q[i >> 2][1][i & 3], qx[i], qx[12 + i]);
    phi20<ISB>(qx, rs, pq);
#pragma unroll
    for (int i = 0; i < (ISB ? 20 : 10); ++i) OPQV(pq[i]);
    if (!ISB) { pq[10] = vzero(); pq[11] = vzero(); }
  }
  f32x4 acc[5];
  {
    unsigned d0, d1;
#pragma unroll
    for (int k = 0; k < 2; ++k) { uswap16(xc[k].x, xc[k].z, xp[4 * k], xp[4 * k + 2]); uswap16(xc[k].y, xc[k].w, xp[4 * k + 1], xp[4 * k + 3]); }
    uswap16(xc[2].x, xc[2].z, xp[8], d0); uswap16(xc[2].y, xc[2].w, xp[9], d1);
  }
#pragma unroll
  for (int ct = 0; ct < 5; ++ct) { acc[ct] = (f32x4){blo(xp[2 * ct]), bhi(xp[2 * ct]), blo(xp[2 * ct + 1]), bhi(xp[2 * ct + 1])}; OPQV(acc[ct]); }
  LSTAMP(7);
  RAWBAR();
  LSTAMP(8);
  {
    bf16x8 f0[NKS], f1[NKS];
#pragma unroll
    for (int ks = 0; ks < NKS; ++ks) f1[ks] = rd(L + k1, 16 * ks);
#pragma unroll
    for (int ks = 0; ks < NKS; ++ks) f0[ks] = rd(L + k0, 16 * ks);
    SB();
    f32x16 n0 = {}, n1 = {};
#pragma unroll
    for (int ks = 0; ks < NKS; ++ks) MF32(n1, f1[ks], asb((v4u){pq[4 * ks], pq[4 * ks + 1], pq[4 * ks + 2], pq[4 * ks + 3]}));
    float id = 0.f;
    const int os = lb + c.oo;
#pragma unroll
    for (int ks = 0; ks < NKS; ++ks) {
      MF32(n0, f0[ks], asb((v4u){pq[4 * ks], pq[4 * ks + 1], pq[4 * ks + 2], pq[4 * ks + 3]}));
      if (ks == NKS - 2) { OPQV(n1); id = __builtin_amdgcn_rcpf(n1[4] + 1e-6f); OPQV(id); }
      if (ks == NKS - 1) {
        const unsigned m0 = pk2(n1[0] * id, n1[1] * id), m1 = pk2(n1[2] * id, n1[3] * id);
        auto r0 = __builtin_amdgcn_permlane32_swap(m0, m0, false, false); auto r1 = __builtin_amdgcn_permlane32_swap(m1, m1, false, false);
        st128(c.rs, xov + 512, os, (v4u){r0[0], r1[0], r0[1], r1[1]});
      }
      SB();
    }
    unsigned d[8];
#pragma unroll
    for (int i = 0; i < 8; ++i) d[i] = pk2(n0[2 * i] * id, n0[2 * i + 1] * id);
    st128(c.rs, ov, os, (v4u){d[0], d[1], d[2], d[3]});
    st128(c.rs, ov + 256, os, (v4u){d[4], d[5], d[6], d[7]});
  }
  RAWBAR();
  LSTAMP(9);
  constexpr int WPD = 32;
  bf16x8 wf[WPD];
#pragma unroll
  for (int k = 0; k < WPD; ++k) wf[k] = rd(wa, k * 1024);
  SB();
  VMWAIT(0);
  __builtin_amdgcn_s_sleep(PD2);
  LSTAMP(10);
  v4u of[20];
  const int oos = lb + R_O + (c.pi * 8 + 4 * c.th + w) * 20480;
#pragma unroll
  for (int s = 0; s < OPF; ++s) { of[s] = ldi(c.rs, l16, oos, s * 1024); SB(); }
  mk = chk128<5>(of);
  sfor<0, 100>([&](auto N) AI {
    constexpr int n = decltype(N)::v, s = n / 5, ct = n % 5, gq = n / 25, u = n % 25;
    if constexpr (u == 0) { fixup<5, gq, ((OPF + 6 * gq) / 5 < 4 ? (OPF + 6 * gq) / 5 : 4)>(c, of, mk, l16, oos, 1024, 2); SB(); }
    if constexpr (n >= WPD && (n & 3) == 0) { OPQV(wf[(n + 3) % WPD]); SB(); }
    MF16(acc[ct], wf[n % WPD], asb(of[s])); SB();
    if constexpr (n % 4 != 3) { constexpr int m = WPD + 3 * (n / 4) + n % 4; if constexpr (m < 100) wf[m % WPD] = (m < 50) ? rd(wa, m * 1024) : rd(wh, (m - 50) * 1024); }
    if constexpr (n % 4 == 3 && OPF + n / 4 < 20) of[OPF + n / 4] = ldi(c.rs, l16, oos, (OPF + n / 4) * 1024);
    if constexpr (gq < 3 && u >= 14 && u < 24 && (u & 1) == 0) { constexpr int q = (u - 14) / 2; mk = mx4(q == 0 ? 0u : mk, of[5 * gq + 5 + q]); OPQV(mk); }
    SB();
  });
  MFIN();
#pragma unroll
  for (int t = 0; t < 5; ++t) OPQV(acc[t]);
  SB();
  LSTAMP(11);
#pragma unroll
  for (int ct = 0; ct < 5; ++ct) { xp[2 * ct] = pk2(acc[ct][0], acc[ct][1]); xp[2 * ct + 1] = pk2(acc[ct][2], acc[ct][3]); }
  x_chunks(xp, xc);
  if (lyr < 3) {
    publish_x(c, lb + LSZ, xrv, xr4, xc);
    RAWBAR();
    publish_stats(c, lb + LSZ, lane, xp);
  } else {
    const rsrc_t ro = __builtin_amdgcn_make_buffer_rsrc((void*)p.out, 0, 2048 * 1280, 0x00020000);
    const int ov2 = t16 * 1280 + 32 * (g & 1) + 16 * (g >> 1), os2 = (128 * c.pi + 64 * c.th + 16 * w) * 1280 + 160 * c.cs;
#pragma unroll
    for (int k = 0; k < 3; ++k) { __builtin_amdgcn_raw_buffer_store_b128(xc[k], ro, (k == 2 && (g & 1)) ? OOR : ov2 + 64 * k, os2, OAUX); asm volatile("s_nop 1" :: "v"(xc[k])); }
  }
  SB();
  LSTAMP(0);
}

#define KM 55
#define Q1 10
#define SL1 1
#define Q2 20
#define SL2 0
#define Q3 20
#define Q4 25
#define WE 12
#define Q5 40
#define HS 10
#define SL3 0
template <bool ISB>
DI void layer_h(const Ctx& c, const int lyr) {
  int lane = lane_id();
  const int w = c.w, t32 = lane & 31, hh = lane >> 5;
  constexpr int NQ = ISB ? 10 : 5, NP = 2 * NQ * 21;
  constexpr int type = ISB ? 1 : 0;
  LAS char* L = c.L;
  int l16 = lane * 16;
  LAS char* wa = L + l16;
  LAS char* sd = wa + w * 25600;
  const int lb = __builtin_amdgcn_readfirstlane(lyr * LSZ);
  int wos = OFF_WO + (type * 8 + c.cs) * WO_SZ + w * 25600;
  int wt0 = OFF_WT + ((1 - type) * 16 + c.pj) * WT_SZ;
  if (lyr == 3) wt0 = OOR - 0x1000000;
  OPQ(wos); OPQ(wt0);
  if (ISB || lyr) bar();
  if (ISB || lyr) row_stats(c, lb, lane);
  if (!ISB && lyr == 0) {
    *(volatile LAS unsigned*)(L + L_GO + 8) = ~c.ep.x;
    v4u u0[13];
#pragma unroll
    for (int k = 0; k < 13; ++k) u0[k] = ldi(c.rs, l16, OFF_WT + c.pj * WT_SZ + w * 12800, 102400 + k * 1024);
    VMWAIT(13);
    int f0 = F_H + ((c.pj * 4 + w) * 16 + c.pi) * 16, f1 = F_R + ((c.pi * 4 + w) * 16 + c.pj) * 16; OPQ(f0); OPQ(f1);
    set_flag(c, lane == 0 ? f0 : (lane == 1 ? f1 : OOR));
    if (w == 0 || w == 3) colsum(c, 0, lane);
    LAS char* ud = wa + 102400 + w * 12800;
#pragma unroll
    for (int k = 0; k < 13; ++k) stw(k < 12 ? ud + k * 1024 : (hh ? L + L_DUMP : ud + 12 * 1024), u0[k]);
  }
  constexpr int NR = ISB ? 2 : 1, PW = NP / 4 + (ISB ? 0 : 1);
  int gv[NR], kvl[NR];
#pragma unroll
  for (int r = 0; r < NR; ++r) {
    const int pn = lane + 64 * r, pc = w * PW + pn; const bool ok = pn < PW && pc < NP;
    const int hf = (pc * 3121) >> 16, zi = w * 11 + lane - 53;
    gv[r] = ok ? pc * 16 : OOR;
    kvl[r] = ok ? L_KV + (pc - hf * 21) * 96 + hf * 8 + (hf >= NQ ? 2016 - NQ * 8 : 0) : ((!ISB && lane >= 53 && zi < 42) ? L_KV + zi * 96 + 40 : L_DUMP);
    OPQV(gv[r]); OPQV(kvl[r]);
  }
  v4u wst[25], up[13], nxt[25];
  auto bg = [&](int n) AI {
    if (n < 25) wst[n] = ldi(c.rs, l16, wos, n * 1024);
    else if (n < 38) up[n - 25] = ldi(c.rs, l16, wt0 + w * 12800, 102400 + (n - 25) * 1024);
    else if (n < 63) nxt[n - 38] = ldi(c.rs, l16, wt0 + w * 25600, (n - 38) * 1024);
    SB();
  };
  if (ISB || lyr) {
#pragma unroll
    for (int n = 0; n < Q1; ++n) { bg(n); __builtin_amdgcn_s_sleep(SL1); }
  }
  bar();
  const unsigned t4 = (unsigned)__builtin_amdgcn_s_memrealtime();
  const int hb = OFF_HB + lyr * 8192;
  if (w == 0) __builtin_amdgcn_raw_buffer_store_b64((v2u){t4, c.ep.x}, c.rs, lane == 0 ? 0 : OOR, hb + (c.pi * 16 + c.pj) * 8, SC1);
  if (!ISB && lyr == 0) {
    wait_flags(c, (lane & 15) * 32 + (lane >> 5) * 8192 + 16, F_A + (c.pj & 7) * 1024, 7);
#pragma unroll
    for (int n = 0; n < Q1; ++n) bg(n);
  }
  if (lyr < 2) poison_all<PSL>(c, lb + 2 * LSZ, ISB ? 7 : 6, l16, lane);
#pragma unroll
  for (int n = Q1; n < Q2; ++n) { bg(n); __builtin_amdgcn_s_sleep(SL2); }
  v4u hv[2];
  const int hvo = w == 3 ? l16 : OOR;
  RAWBAR();
#pragma unroll
  for (int q = 0; q < 2; ++q) hv[q] = ld128(c.rs, hvo, hb + q * 1024);
#pragma unroll
  for (int n = Q2; n < Q3; ++n) bg(n);
  RAWBAR();
  const unsigned t6 = (unsigned)__builtin_amdgcn_s_memrealtime();
  v4u kg[NR][16];
  const int gs = lb + R_KV + c.pj * 16 * KVSLOT + 512;
  if (!ISB && lyr == 0) wait_flags(c, l16, F_H + c.pj * 1024, 7);
  {
    volatile LAS unsigned* go = (volatile LAS unsigned*)(L + L_GO) + (lyr & 1);
    int lag = 0;
    if (w == 3) {
      NOUNROLL for (unsigned spins = 0; spins < 64; ++spins) {
        bool ok = true; lag = 0;
#pragma unroll
        for (int q = 0; q < 2; ++q) { ok &= hv[q].y == c.ep.x && hv[q].w == c.ep.x; lag = max(max(lag, (int)(hv[q].x - t4)), (int)(hv[q].z - t4)); }
        if (__all(ok)) break;
        RCOUNT(5);
#pragma unroll
        for (int q = 0; q < 2; ++q) hv[q] = ld128(c.rs, hvo, hb + q * 1024);
        lag = 0;
      }
#pragma unroll
      for (int d = 32; d >= 1; d >>= 1) lag = max(lag, __shfl_xor(lag, d));
      lag = min(lag, 400);
      *go = ((unsigned)(lyr + 1) << 16) | (unsigned)lag;
    }
#pragma unroll
  for (int k = 0; k < WE; ++k) { if (Q3 + k < Q4) bg(Q3 + k); stw(sd + k * 1024, wst[k]); SB(); }
    if (w != 3) {
      unsigned v = 0;
      NOUNROLL for (unsigned spins = 0; spins < 4096; ++spins) { v = *go; if ((v >> 16) == (unsigned)(lyr + 1)) break; __builtin_amdgcn_s_sleep(1); }
      lag = v & 0x1FF;
    }
    lag = __builtin_amdgcn_readfirstlane(lag) + KM;
    NOUNROLL while ((int)((unsigned)__builtin_amdgcn_s_memrealtime() - t6) < lag) __builtin_amdgcn_s_sleep(1);
  }
#pragma unroll
  for (int n = 0; n < 16 * NR; ++n) { kg[n % NR][n / NR] = ld128(c.rs, gv[n % NR], gs + (n / NR) * KVSLOT); SB(); }
  {
    f32x4 sum[NR];
    bool redo = false;
#pragma unroll
    for (int i = 0; i < 16; ++i) {
      unsigned m = 0;
#pragma unroll
      for (int r = 0; r < NR; ++r) { m = mx4(m, kg[r][i]); sum[r] = i ? sum[r] + __builtin_bit_cast(f32x4, kg[r][i]) : __builtin_bit_cast(f32x4, kg[r][i]); OPQV(sum[r]); }
      if (__builtin_expect(__any(m == POISON), 0)) {
        redo = true;
        int gs2 = gs; OPQ(gs2);
#pragma unroll
        for (int r = 0; r < NR; ++r) kg[r][i] = ld128(c.rs, gv[r], gs2 + i * KVSLOT);
      }
    }
    if (__builtin_expect(redo, 0)) {
      NOUNROLL for (unsigned spins = 0; spins < SPIN_CAP; ++spins) {
        RCOUNT(1);
        bool any = false;
        int gs2 = gs; OPQ(gs2);
        __builtin_amdgcn_s_waitcnt(0x0F70);
#pragma unroll
        for (int i = 0; i < 16; ++i)
#pragma unroll
          for (int r = 0; r < NR; ++r) sum[r] = i ? sum[r] + __builtin_bit_cast(f32x4, kg[r][i]) : __builtin_bit_cast(f32x4, kg[r][i]);
        float t = sum[0][1] + sum[0][3];
        if (NR == 2) t += sum[NR - 1][1] + sum[NR - 1][3];
        if (!__any(t != t)) break;
#pragma unroll
        for (int i = 0; i < 16; ++i) {
          unsigned m = 0;
#pragma unroll
          for (int r = 0; r < NR; ++r) m = mx4(m, kg[r][i]);
          if (__any(m == POISON)) {
            any = true;
#pragma unroll
            for (int r = 0; r < NR; ++r) kg[r][i] = ld128(c.rs, gv[r], gs2 + i * KVSLOT);
          }
        }
        if (!any) break;
      }
      __builtin_amdgcn_s_waitcnt(0x0F70);
    }
#pragma unroll
    for (int r = 0; r < NR; ++r) *(LAS v2u*)(L + kvl[r]) = (v2u){pk2(sum[r][0], sum[r][1]), pk2(sum[r][2], sum[r][3])};
  }
  SB();
  bar();
#pragma unroll
  for (int k = WE; k < 25; ++k) stw(sd + k * 1024, wst[k]);
  SB();
  if (!ISB && lyr == 0 && w == 0) wait_flags(c, l16, F_R + c.pi * 1024, 7);
  bar();
  if (lyr == 3) return;
#pragma unroll
  for (int n = Q4; n < Q5; ++n) bg(n);
  {
    LAS char* ud = wa + 102400 + w * 12800;
    LAS char* ul = hh ? L + L_DUMP : ud + 12 * 1024;
#pragma unroll
    for (int k = 0; k < 13; ++k) stw(k < 12 ? ud + k * 1024 : ul, up[k]);
  }
  SB();
  __builtin_amdgcn_s_sleep(HS);
  if (!ISB && lyr == 0 && w < 2) colsum(c, 1, lane);
#pragma unroll
  for (int n = Q5; n < 63; ++n) { bg(n); __builtin_amdgcn_s_sleep(SL3); }
  RAWBAR();
#pragma unroll
  for (int k = 0; k < 25; ++k) stw(sd + k * 1024, nxt[k]);
  SB();
}

DI void set_ctx(Ctx& c, const Params& p, int b, char* smem, int wv = -1) {
  c.L = (LAS char*)smem;
  c.rs = __builtin_amdgcn_make_buffer_rsrc(p.ws, 0, WS_USED, 0x00020000);
  const int xcd = b & 7, r = b >> 3;
  c.pi = 4 * (xcd >> 1) + (r & 3); c.pj = 8 * (xcd & 1) + (r >> 2); c.th = c.pj >> 3; c.cs = c.pj & 7;
  c.lane = threadIdx.x & 63; c.w = wv >= 0 ? wv : (__builtin_amdgcn_readfirstlane(threadIdx.x >> 6) & 3);
  c.xo = R_X + (4 * c.pi + 2 * c.th + (c.w >> 1)) * 40960 + (c.w & 1) * 1024;
  c.oo = R_O + (c.pi * 8 + 2 * c.w) * 20480 + 5 * c.pj * 256;
  c.ko = R_KV + (c.pj * 16 + c.pi) * KVSLOT;
  OPQ(c.xo); OPQ(c.oo); OPQ(c.ko);
}
constexpr int CODE_KB = 48;
__global__ void __launch_bounds__(512, 2) mega(Params p) {
  extern __shared__ __attribute__((aligned(16))) char smem[];
  unsigned pf;
  {
    unsigned long long pc; asm volatile("s_getpc_b64 %0" : "=s"(pc));
    const unsigned ln = min(((blockIdx.x >> 3) & 3u) * 128u + (threadIdx.x >> 2), (unsigned)(CODE_KB * 8 - 1));
    asm volatile("global_load_dword %0, %1, %2" : "=v"(pf) : "v"(ln * 128u), "s"(pc & ~127ull) : "memory");
  }
  Ctx c;
  set_ctx(c, p, blockIdx.x, smem);
  const int w8 = __builtin_amdgcn_readfirstlane(threadIdx.x >> 6);
  LAS char* L = c.L;
  const int lane = c.lane, w = c.w, t16 = lane & 15, g = lane >> 4, q4 = t16 >> 2, p4 = lane & 3;
  int l16 = lane * 16;
  { const int i_ = threadIdx.x; ZST(i_); }
  v2u ep;
  if (w8 < 4) {
    STAMP(0); CSTAMP(120);
    const rsrc_t rx = __builtin_amdgcn_make_buffer_rsrc((void*)p.x, 0, 2048 * 1280, 0x00020000);
    const int xa = t16 * 1280 + 32 * (g & 1) + 16 * (g >> 1);
    v4u xc[3];
#pragma unroll
    for (int k = 0; k < 3; ++k) xc[k] = __builtin_amdgcn_raw_buffer_load_b128(rx, (k == 2 && (g & 1)) ? OOR : xa + 64 * k, (128 * c.pi + 64 * c.th + 16 * w) * 1280 + 160 * c.cs, 0);
    ep = ld64(c.rs, 0, OFF_CTRL);
    const int xv = x_voff(c, lane);
    SB();
    publish_x(c, 0, xv, (g & 1) ? OOR : xv, xc);
    unsigned xp[10], d0, d1;
#pragma unroll
    for (int k = 0; k < 2; ++k) { xp[4 * k] = xc[k].x; xp[4 * k + 1] = xc[k].y; xp[4 * k + 2] = xc[k].z; xp[4 * k + 3] = xc[k].w; }
    uswap16(xc[2].x, xc[2].z, xp[8], d0); uswap16(xc[2].y, xc[2].w, xp[9], d1);
    publish_stats(c, 0, lane, xp);
    c.ep = next_epoch(ep);
    int f0 = F_X + (((c.pi * 2 + c.th) * 4 + w) * 8 + c.cs) * 16; OPQ(f0);
    STAMP(101);
    DRAIN();
    set_flag(c, lane == 0 ? f0 : OOR);
    STAMP(102);
    __builtin_amdgcn_s_sleep(8);
    wait_flags(c, t16 * 16, F_X + (c.pi * 4 + w) * 256, 4);
    STAMP(103);
    row_stats(c, 0, lane);
  } else {
    LAS char* S = L + L_Q + w * 12800;
    const int kind = w == 2 ? 2 : (w == 3 ? (c.pi < 8 ? c.pi >> 2 : 3) : w), kb = w == 3 ? 16 + (c.pi & 3) : c.pi;
    const rsrc_t rw = __builtin_amdgcn_make_buffer_rsrc((void*)(kind == 2 ? (c.th ? p.wo[1] : p.wo[0]) : (kind ? p.wqkv[1] : p.wqkv[0])), 0, kind == 3 ? 0 : (kind == 2 ? 640 * 1280 : 640 * 3840), 0x00020000);
    const int vq = kind == 2 ? (t16 < 10 ? g * 1280 + t16 * 16 : OOR) : (t16 < 15 ? g * 3840 + t16 * 16 + (t16 >= 5 ? 1200 : 0) + (t16 >= 10 ? 1200 : 0) : OOR);
    const int so = kind == 2 ? c.cs * 160 + 40 * kb * 1280 : c.pj * 80 + 32 * kb * 3840, st = kind == 2 ? 4 * 1280 : 4 * 3840;
    if (kind) __builtin_amdgcn_s_sleep(PW0);
    v2u cp[8];
    {
      v4u cw[10];
#pragma unroll
      for (int k = 0; k < 10; ++k) cw[k] = __builtin_amdgcn_raw_buffer_load_b128(rw, vq, (k < 8 || kind == 2) ? so + k * st : OOR, 0);
      ep = ld64(c.rs, 0, OFF_CTRL);
      if (kind == 1) {
        const rsrc_t rp = __builtin_amdgcn_make_buffer_rsrc((void*)p.wphi, 0, 32 * 800, 0x00020000);
        const int d = lane >> 3, q8 = lane & 7;
#pragma unroll
        for (int k = 0; k < 8; ++k) cp[k] = __builtin_amdgcn_raw_buffer_load_b64(rp, (d + 8 * (k & 3) < 20 && q8 < 5) ? d * 40 + q8 * 8 : OOR, (2 * c.pj + (k >> 2)) * 800 + (k & 3) * 320, 0);
      }
      SB();
#pragma unroll
      for (int k = 0; k < 10; ++k) *(LAS v4u*)(S + l16 + k * 1024) = cw[k];
    }
    c.ep = next_epoch(ep);
    if (w == 0 && lane < 24) *(LAS unsigned*)(L + L_ZERO + 4 * lane) = 0u;
    if (kind == 1) {
#pragma unroll
        for (int k = 0; k < 8; ++k) *(LAS v2u*)(S + 8192 + lane * 8 + k * 512) = cp[k];
        LAS char* pa = S + 8192 + 64 * (8 * g + q4) + 8 * p4;
        LAS char* pr = S + t16 * 256;
        LAS char* pb = pr + (g < 3 ? g * 16 : 0); LAS char* pb2 = pb + (g == 2 ? 0 : 8);
        f32x4 o0[8], o1[8];
#pragma unroll
        for (int u = 0; u < 8; ++u) {
          const bf16x8 f0 = tr2(pa + (u & 1) * 2048, 0, 256), f1 = tr2(pa + (u & 1) * 2048, 32, 288);
          const v2u lo = *(LAS v2u*)(pb + (u & 3) * 40 + (u >> 2) * 4096), hi = *(LAS v2u*)(pb2 + (u & 3) * 40 + (u >> 2) * 4096);
          const bf16x8 bb = asb((v4u){lo.x, lo.y, hi.x, hi.y});
          o0[u] = mfma16(f0, bb, (f32x4){}); o1[u] = mfma16(f1, bb, (f32x4){});
        }
        SB();
#pragma unroll
        for (int u = 0; u < 8; ++u) {
          *(LAS v2u*)(pr + 8 * g + (u & 3) * 40 + (u >> 2) * 4096) = (v2u){pk2(o0[u][0], o0[u][1]), pk2(o0[u][2], o0[u][3])};
          *(LAS v2u*)(g == 0 ? pr + 32 + (u & 3) * 40 + (u >> 2) * 4096 : S + 12288) = (v2u){pk2(o1[u][0], o1[u][1]), pk2(o1[u][2], o1[u][3])};
        }
        SB();
    }
    v4u f[8];
    if (kind != 2) {
      LAS char* pt = S + (8 * g + q4) * 256 + (p4 >> 1) * 40;
      const int hd = p4 >> 1;
#pragma unroll
      for (int T = 0; T < 8; ++T) {
        const int sq = T < 5 ? T : ((p4 & 1) ? (T < 7 ? T - 2 : 2) : T - 5);
        const int qd = sq + hd >= 5 ? sq + hd - 5 : sq + hd;
        f[T] = __builtin_bit_cast(v4u, tr2(pt + (T < 5 ? 80 + 80 * (p4 & 1) : 0) + 8 * qd, 0, 1024));
      }
    } else {
#pragma unroll
      for (int k = 0; k < 8; ++k) {
        const int gc = min(4 * k + g, 24), rg = (gc * 13) >> 6, ct = gc - rg * 5;
        f[k] = __builtin_bit_cast(v4u, tr2(S + (8 * rg + q4) * 256 + 32 * ct + 8 * p4, 0, 1024));
      }
    }
    SB();
    if (kind == 2) {
#pragma unroll
      for (int k = 0; k < 7; ++k) {
        const int gi = 4 * k + g, rg = (gi * 13) >> 6, ct = gi - rg * 5, RG = 5 * c.pi + rg;
        st128r(c.rs, gi < 25 ? (((RG >> 2) * 5 + ct) * 64 + (RG & 3) * 16 + t16) * 16 : OOR, OFF_WO + c.pj * WO_SZ, f[k]);
      }
      DRAIN();
      int f0 = F_A + (c.pj * 32 + c.pi) * 32 + 16; OPQ(f0);
      set_flag(c, lane == 0 ? f0 : OOR);
    }
    v4u im[25];
    volatile LAS unsigned* go = (volatile LAS unsigned*)(L + L_GO + 8);
    NOUNROLL for (int pass = 0; pass < 2; ++pass) {
      if (pass == 1) {
        if (w == 2) {
          __builtin_amdgcn_s_sleep(PW1);
          wait_flags(c, min(lane, 19) * 32, F_A + c.pj * 1024, 6);
          go[0] = c.ep.x; go[1] = ~c.ep.y;
        } else {
          NOUNROLL for (unsigned spins = 0; spins < (SPIN_CAP << 4); ++spins) { if (go[0] == c.ep.x && go[1] == ~c.ep.y) break; __builtin_amdgcn_s_sleep(1); }
          asm volatile("" ::: "memory");
        }
        STAMP(106);
#pragma unroll
        for (int k = 0; k < 25; ++k) im[k] = ldi(c.rs, l16, OFF_WT + c.pj * WT_SZ + w * 25600, k * 1024);
        SB();
#pragma unroll
        for (int k = 0; k < 25; ++k) *(LAS v4u*)(L + w * 25600 + l16 + k * 1024) = im[k];
        if (w == 1 && lane < 2) *(LAS unsigned*)(L + L_GO + 4 * lane) = 0u;
        STAMP(108);
        bar();
      }
      if (pass == kind) {
        const int tv7 = (t16 & 4) ? OOR : g * 128 + (t16 >> 3) * 64 + (t16 & 3) * 16, ib = OFF_WT + (16 * kind + c.pj) * WT_SZ;
#pragma unroll
        for (int T = 0; T < 8; ++T) st128r(c.rs, T < 7 ? l16 : tv7, ib + (T < 5 ? (kb * 5 + T) * 1024 : L_Q + kb * 2560 + (T - 5) * 1024), f[T]);
        float sm[8];
#pragma unroll
        for (int T = 0; T < 8; ++T) sm[T] = xsum32(xsum16(sum2(f[T].x, sum2(f[T].y, sum2(f[T].z, sum2(f[T].w, 0.f))))));
        st128r(c.rs, g < 2 ? t16 * 32 + g * 16 : OOR, OFF_CS + (kind * 16 + c.pj) * 10240 + kb * 512, g ? (v4u){f2u(sm[4]), f2u(sm[5]), f2u(sm[6]), f2u(sm[7])} : (v4u){f2u(sm[0]), f2u(sm[1]), f2u(sm[2]), f2u(sm[3])});
        if (kind == 0) {
          STAMP(104);
          DRAIN();
          int f0 = F_A + (c.pj * 32 + kb) * 32; OPQ(f0);
          set_flag(c, lane == 0 ? f0 : OOR);
          STAMP(105);
        }
      }
    }
    int bq = c.oo + 1024, dq = c.oo + 1024 + 20480 - 256; OPQ(bq); OPQ(dq);
    poison128(c.rs, l16, c.oo); poison128(c.rs, l16, c.oo + 20480);
    poison128(c.rs, lane < 32 ? l16 + (lane < 16 ? bq : dq) : OOR, 0);
    poison128(c.rs, l16, c.ko + 512 + w * 1024);
    SB();
    poison_all<0>(c, LSZ, 7, l16, lane);
  }
  asm volatile("" :: "v"(pf));
  int role = w8; OPQ(role);
  if (role < 4) {
    for (int it = 0; it < 2; ++it) {
      layer_c<false>(c, p, 2 * it);
      layer_c<true>(c, p, 2 * it + 1);
    }
    if (blockIdx.x == 0 && w == 0) set_flag(c, lane_id() == 0 ? 0 : OOR);
    STAMP(100); CSTAMP(121);
  } else {
    __builtin_amdgcn_s_setprio(HPRIO);
    for (int it = 0; it < 2; ++it) {
      layer_h<false>(c, 2 * it);
      layer_h<true>(c, 2 * it + 1);
    }
  }
  DUMP();
}

extern "C" void kernel_launch(void* const* d_in, const int* in_sizes, int n_in, void* d_out, int out_size, void* d_ws, size_t ws_size, hipStream_t stream) {
  Params p;
  p.x = (const unsigned short*)d_in[0];
  p.wqkv[0] = (const unsigned short*)d_in[1]; p.wo[0] = (const unsigned short*)d_in[2];
  p.wqkv[1] = (const unsigned short*)d_in[4]; p.wphi = (const unsigned short*)d_in[5]; p.wo[1] = (const unsigned short*)d_in[6];
  p.ws = (unsigned char*)d_ws; p.out = (unsigned short*)d_out;
  (void)hipFuncSetAttribute((const void*)mega, hipFuncAttributeMaxDynamicSharedMemorySize, LDS_BYTES);
  mega<<<256, 512, LDS_BYTES, stream>>>(p);
}
```

```cpp
#include <hip/hip_runtime.h>
#include <hip/hip_bf16.h>
#include <stdint.h>

typedef unsigned v4u __attribute__((ext_vector_type(4)));
typedef unsigned v2u __attribute__((ext_vector_type(2)));
typedef short bf16x8 __attribute__((ext_vector_type(8)));
typedef short s16x4 __attribute__((ext_vector_type(4)));
typedef float f32x2 __attribute__((ext_vector_type(2)));
typedef float f32x4 __attribute__((ext_vector_type(4)));
typedef float f32x16 __attribute__((ext_vector_type(16)));
typedef __bf16 bf16x2v __attribute__((ext_vector_type(2)));
#define LAS __attribute__((address_space(3)))
#define GAS __attribute__((address_space(1)))
#define DI __device__ __forceinline__
#define AI __attribute__((always_inline))
typedef __amdgpu_buffer_rsrc_t rsrc_t;

constexpr int SC1 = 16;
constexpr unsigned POISON = 0xFFFFFFFFu;
constexpr unsigned SPIN_CAP = 1u << 16;
constexpr int OOR = 0x7FFF0000;

constexpr int XB_SZ = 2048 * 640 * 2;
constexpr int KVSLOT = 8192;
constexpr int R_X = 0, R_O = XB_SZ, R_KV = 2 * XB_SZ;
constexpr int LSZ = 2 * XB_SZ + 256 * KVSLOT;
constexpr int WT_SZ = 153600, WO_SZ = 102400;
constexpr int OFF_WT = 4 * LSZ;
constexpr int OFF_CS = OFF_WT + 2 * 16 * WT_SZ;
constexpr int OFF_WO = OFF_CS + 32 * 10240;
constexpr int OFF_HB = OFF_WO + 2 * 8 * WO_SZ;
constexpr int OFF_CTRL = OFF_HB + 32768;
constexpr int F_X = 4096, F_R = F_X + 16384, F_H = F_R + 16384, F_A = F_H + 16384;
constexpr int WS_USED = OFF_CTRL + F_A + 16384;

constexpr int L_Q = 102400, L_MISC = 153600;
constexpr int L_KV = L_MISC;
constexpr int L_ZERO = L_KV + 4032;
constexpr int L_DUMP = L_ZERO + 96;
constexpr int L_CS = L_DUMP + 16;
constexpr int L_ST = L_CS + 2048;
constexpr int L_GO = L_ST + 1024;
constexpr int LDS_BYTES = 163840;

constexpr int XPN = 16, XRA = 10, XRN = 12, XE = 4, RQ = 3;
constexpr int PAN = 8, B4N = 69, OPF = 10;
constexpr int PD1 = 3, PD2 = 3, KM = 55, PW0 = 12;
constexpr int Q1 = 10, Q2 = 20, Q4 = 25, Q5 = 40, WE = 12, SL1 = 1, HS = 10;

#define OPQ(x) do { x = __builtin_amdgcn_readfirstlane(x); asm volatile("" : "+s"(x)); } while (0)
#define OPQV(x) asm volatile("" : "+v"(x))
#define SB() __builtin_amdgcn_sched_barrier(0)
#define DRAIN() asm volatile("s_waitcnt vmcnt(0)" ::: "memory")
#define VMWAIT(n) asm volatile("s_waitcnt vmcnt(%0)" :: "i"(n) : "memory")
#define NOUNROLL _Pragma("nounroll")
DI v4u ld128(rsrc_t rs, int voff, int soff) { return __builtin_amdgcn_raw_buffer_load_b128(rs, voff, soff, SC1); }
DI v2u ld64(rsrc_t rs, int voff, int soff) { return __builtin_amdgcn_raw_buffer_load_b64(rs, voff, soff, SC1); }
DI v4u ldi(rsrc_t rs, int& voff, int soff, int coff) { OPQV(voff); return ld128(rs, voff + (coff & 4095), soff + (coff & ~4095)); }
DI unsigned np(unsigned u) { return u < 0xFFFFFFFEu ? u : 0xFFFFFFFEu; }
DI void st128r(rsrc_t rs, int voff, int soff, v4u v) { __builtin_amdgcn_raw_buffer_store_b128(v, rs, voff, soff, SC1); asm volatile("s_nop 1" :: "v"(v)); }
DI void st128(rsrc_t rs, int voff, int soff, v4u v) { st128r(rs, voff, soff, (v4u){v.x, np(v.y), v.z, np(v.w)}); }
DI void st64(rsrc_t rs, int voff, int soff, v2u v) { v2u q = {v.x, np(v.y)}; __builtin_amdgcn_raw_buffer_store_b64(q, rs, voff, soff, SC1); }
DI void poison128(rsrc_t rs, int voff, int soff) { st128r(rs, voff, soff, (v4u){POISON, POISON, POISON, POISON}); }
DI unsigned pk2(float a, float b) { f32x2 v = {a, b}; return __builtin_bit_cast(unsigned, __builtin_convertvector(v, bf16x2v)); }
DI float u2f(unsigned u) { return __builtin_bit_cast(float, u); }
DI unsigned f2u(float f) { return __builtin_bit_cast(unsigned, f); }
DI float rbf(float a) { return u2f(pk2(a, a) & 0xFFFF0000u); }
DI float blo(unsigned u) { return u2f(u << 16); }
DI float bhi(unsigned u) { return u2f(u & 0xFFFF0000u); }
DI float sum2(unsigned u, float acc) { return __builtin_amdgcn_fdot2_f32_bf16(__builtin_bit_cast(bf16x2v, u), __builtin_bit_cast(bf16x2v, 0x3F803F80u), acc, false); }
DI unsigned umax3(unsigned a, unsigned b, unsigned c) { return max(max(a, b), c); }
DI unsigned mx4(unsigned m, v4u v) { return umax3(m, v.y, v.w); }
DI bf16x8 tr2(LAS char* p, int off1, int off2) {
  s16x4 a = __builtin_amdgcn_ds_read_tr16_b64_v4i16((LAS s16x4*)(p + off1));
  s16x4 b = __builtin_amdgcn_ds_read_tr16_b64_v4i16((LAS s16x4*)(p + off2));
  return __builtin_shufflevector(a, b, 0, 1, 2, 3, 4, 5, 6, 7);
}
DI bf16x8 rd(LAS char* p, int off) { return *(LAS bf16x8*)(p + off); }
DI f32x16 mfma32(bf16x8 a, bf16x8 b, f32x16 c) { return __builtin_amdgcn_mfma_f32_32x32x16_bf16(a, b, c, 0, 0, 0); }
DI f32x4 mfma16(bf16x8 a, bf16x8 b, f32x4 c) { return __builtin_amdgcn_mfma_f32_16x16x32_bf16(a, b, c, 0, 0, 0); }
DI bf16x8 asb(v4u v) { return __builtin_bit_cast(bf16x8, v); }
DI float xsum32(float v) { unsigned u = f2u(v); auto r = __builtin_amdgcn_permlane32_swap(u, u, false, false); return u2f(r[0]) + u2f(r[1]); }
DI float xsum16(float v) { unsigned u = f2u(v); auto r = __builtin_amdgcn_permlane16_swap(u, u, false, false); return u2f(r[0]) + u2f(r[1]); }
DI int lane_id() { int l; asm volatile("v_mbcnt_lo_u32_b32 %0, -1, 0\n\tv_mbcnt_hi_u32_b32 %0, -1, %0" : "=v"(l)); return l; }
DI unsigned vzero() { unsigned z; asm volatile("v_mov_b32 %0, 0" : "=v"(z)); return z; }
DI unsigned vone() { unsigned z; asm volatile("v_mov_b32 %0, 0x3f80" : "=v"(z)); return z; }
DI void bar() { asm volatile("s_waitcnt lgkmcnt(0)" ::: "memory"); __builtin_amdgcn_s_barrier(); asm volatile("" ::: "memory"); }
#define MF16(acc, a, b) asm volatile("v_mfma_f32_16x16x32_bf16 %0, %1, %2, %0" : "+v"(acc) : "v"(a), "v"(b))
#define MFIN() asm volatile("s_nop 7\n\ts_nop 1" ::: "memory")
#define MF32(acc, a, b) do { acc = mfma32(a, b, acc); OPQV(acc); SB(); } while (0)
DI void stw(LAS char* p, v4u& v) { *(LAS v4u*)p = v; }
#define RAWBAR() do { asm volatile("" ::: "memory"); SB(); __builtin_amdgcn_s_barrier(); SB(); asm volatile("" ::: "memory"); } while (0)
template <int I> struct IC { static constexpr int v = I; };
template <int B, int E, class F> DI void sfor(F&& f) { if constexpr (B < E) { f(IC<B>{}); sfor<B + 1, E>(f); } }

struct Params {
  const unsigned short* x; const unsigned short* wqkv[2]; const unsigned short* wo[2]; const unsigned short* wphi;
  unsigned char* ws; unsigned short* out;
};
struct Ctx { rsrc_t rs; LAS char* L; int pi, pj, th, cs, w, lane; int xo, oo, ko; v2u ep; };

template <int N> DI unsigned chk128(const v4u* x) { unsigned m = 0;
#pragma unroll
  for (int k = 0; k < N; ++k) m = mx4(m, x[k]);
  return m; }
template <int N, int G0, int G1>
DI void fixup(const Ctx& c, v4u* x, unsigned mk, int voff, int soff, int stride) {
  if (__builtin_expect(__any(mk == POISON), 0)) {
    NOUNROLL for (unsigned spins = 0; spins < SPIN_CAP; ++spins) {
      OPQ(soff);
      bool bad[G1 - G0];
#pragma unroll
      for (int q = G0; q < G1; ++q) bad[q - G0] = __any(chk128<N>(x + N * q) == POISON);
      SB();
#pragma unroll
      for (int q = G0; q < G1; ++q) {
        if (bad[q - G0]) {
#pragma unroll
          for (int k = N * q; k < N * q + N; ++k) x[k] = ld128(c.rs, voff, soff + k * stride);
        }
      }
      if (!bad[0]) break;
    }
    __builtin_amdgcn_s_waitcnt(0x0F70);
  }
}

template <bool ISB>
DI void phi20(const float* v, float rs, unsigned* out) {
  const float rl = rs * 1.44269504088896f;
  if constexpr (!ISB) {
    float r[20];
#pragma unroll
    for (int i = 0; i < 20; ++i) {
      const float y = v[i] * rl;
      r[i] = fmaf(__builtin_amdgcn_fmed3f(y, 1.44269504e-6f, __builtin_inff()), 0.693147180559945f, __builtin_amdgcn_fmed3f(__builtin_amdgcn_exp2f(y), 0.f, 1.f));
    }
#pragma unroll
    for (int i = 0; i < 10; ++i) out[i] = pk2(r[2 * i], r[2 * i + 1]);
  } else {
    float mx = __builtin_amdgcn_fmed3f(v[0], v[1], __builtin_inff()), mn = __builtin_amdgcn_fmed3f(v[0], v[1], -__builtin_inff());
#pragma unroll
    for (int i = 2; i < 20; i += 2) { mx = fmaxf(fmaxf(mx, v[i]), v[i + 1]); mn = fminf(fminf(mn, v[i]), v[i + 1]); }
    const float cp = -mx * rl, cn = mn * rl;
    const f32x2 rl2 = {rl, rl}, cp2 = {cp, cp}, cn2 = {cn, cn};
    f32x2 ep[10], en[10], sp, sn;
#pragma unroll
    for (int i = 0; i < 10; ++i) {
      const f32x2 a = {v[2 * i], v[2 * i + 1]};
      const f32x2 yp = __builtin_elementwise_fma(a, rl2, cp2), yn = __builtin_elementwise_fma(a, -rl2, cn2);
      ep[i] = (f32x2){__builtin_amdgcn_exp2f(yp[0]), __builtin_amdgcn_exp2f(yp[1])};
      en[i] = (f32x2){__builtin_amdgcn_exp2f(yn[0]), __builtin_amdgcn_exp2f(yn[1])};
      sp = i ? sp + ep[i] : ep[i]; sn = i ? sn + en[i] : en[i];
    }
    const float ip = __builtin_amdgcn_rcpf(sp[0] + sp[1]), in = __builtin_amdgcn_rcpf(sn[0] + sn[1]);
#pragma unroll
    for (int i = 0; i < 10; ++i) { const f32x2 a = ep[i] * ip, b = en[i] * in; out[i] = pk2(a[0], a[1]); out[10 + i] = pk2(b[0], b[1]); }
  }
}
DI void swap16(float a, float b, float& lo, float& hi) { auto r = __builtin_amdgcn_permlane16_swap(f2u(a), f2u(b), false, false); lo = u2f(r[0]); hi = u2f(r[1]); }

DI void poison_all(const Ctx& c, int lb, int n, int l16, int lane) {
  const int odd = c.cs & 1, s0 = (5 * c.cs + odd) >> 1;
  int xf = c.xo + (s0 * 2 + (c.w & 1)) * 1024, a = c.xo + (odd ? s0 - 1 : s0 + 2) * 2048 + odd * 512, b = c.oo + 1024 - 512, d = c.oo + 1024 + 20480 - 768;
  OPQ(xf); OPQ(a); OPQ(b); OPQ(d);
  poison128(c.rs, l16, lb + xf); poison128(c.rs, l16 + 1024, lb + xf);
  poison128(c.rs, l16, lb + c.oo); poison128(c.rs, l16, lb + c.oo + 20480);
  poison128(c.rs, l16 + (lane < 32 ? a : (lane < 48 ? b : d)), lb);
  poison128(c.rs, l16, lb + c.ko + c.w * 1024);
  if (n > 6) poison128(c.rs, l16, lb + c.ko + c.w * 1024 + 4096);
}
DI int x_piece(const Ctx& c, int ct) { const int ch = 10 * c.cs + 2 * ct; return c.xo + (ch >> 2) * 2048 + (ch & 3) * 256; }
DI void uswap16(unsigned a, unsigned b, unsigned& lo, unsigned& hi) { auto r = __builtin_amdgcn_permlane16_swap(a, b, false, false); lo = r[0]; hi = r[1]; }
DI void x_chunks(const unsigned* xp, v4u* ch) {
#pragma unroll
  for (int k = 0; k < 3; ++k) {
    unsigned a0, a1, b0, b1;
    uswap16(xp[4 * k], xp[k < 2 ? 4 * k + 2 : 8], a0, b0); uswap16(xp[4 * k + 1], xp[k < 2 ? 4 * k + 3 : 9], a1, b1);
    ch[k] = (v4u){a0, a1, b0, b1};
  }
}
DI int x_voff(const Ctx& c, int lane) { int d = (c.cs & 1) ? 1536 : 512; OPQ(d); return ((lane >> 4) & 1) * d + (lane >> 5) * 256 + (lane & 15) * 16; }
DI void publish_x(const Ctx& c, int lb, int xv, int xv4, const v4u* ch) {
#pragma unroll
  for (int k = 0; k < 3; ++k) st128(c.rs, k < 2 ? xv : xv4, lb + x_piece(c, 2 * k), ch[k]);
}
DI void publish_stats(const Ctx& c, int lb, int lane, const unsigned* xp) {
  float s = 0.f;
#pragma unroll
  for (int i = 0; i < 10; ++i) s = sum2(xp[i], s);
  s = xsum32(xsum16(s));
  const float m = s * (1.f / 80.f);
  f32x2 q2 = {0.f, 0.f};
#pragma unroll
  for (int i = 0; i < 10; ++i) { const f32x2 d = (f32x2){blo(xp[i]), bhi(xp[i])} - m; q2 = __builtin_elementwise_fma(d, d, q2); }
  const float q = xsum32(xsum16(q2[0] + q2[1]));
  st64(c.rs, lane < 16 ? lane * 8 : OOR, lb + c.ko + c.w * 128, (v2u){f2u(s), f2u(q)});
}
DI v2u next_epoch(v2u e) { unsigned x = __builtin_amdgcn_readfirstlane(e.x) + 1u, y = __builtin_amdgcn_readfirstlane(e.y) + (x == 0u); return (v2u){x, y}; }
DI void wait_flags(const Ctx& c, int voff, int soff) {
  NOUNROLL for (unsigned spins = 0; spins < SPIN_CAP; ++spins) {
    const v2u f = ld64(c.rs, voff, OFF_CTRL + soff);
    if (__all(f.x == c.ep.x && f.y == c.ep.y)) break;
    __builtin_amdgcn_s_sleep(2);
  }
  asm volatile("" ::: "memory");
}
DI void set_flag(const Ctx& c, int voff) { __builtin_amdgcn_raw_buffer_store_b64(c.ep, c.rs, voff, OFF_CTRL, SC1); }
DI void row_stats(const Ctx& c, int lb, int lane) {
  const int w = c.w, t32 = lane & 31, hh = lane >> 5;
  v2u stv[4];
  const int sv = hh * (4 * 16 * KVSLOT) + t32 * 8; int ss = lb + R_KV + ((w >> 1) * 128 + c.pi) * KVSLOT + (w & 1) * 256;
  NOUNROLL for (unsigned spins = 0; spins < SPIN_CAP; ++spins) {
#pragma unroll
    for (int q = 0; q < 4; ++q) stv[q] = ld64(c.rs, sv, ss + q * 16 * KVSLOT);
    if (!__any(max(umax3(stv[0].y, stv[1].y, stv[2].y), stv[3].y) == POISON)) break;
    __builtin_amdgcn_s_sleep(4);
  }
  const float mean = xsum32((u2f(stv[0].x) + u2f(stv[1].x)) + (u2f(stv[2].x) + u2f(stv[3].x))) * (1.f / 640.f);
  float m2 = 0.f;
#pragma unroll
  for (int q = 0; q < 4; ++q) { const float d = u2f(stv[q].x) * (1.f / 80.f) - mean; m2 += fmaf(80.f * d, d, u2f(stv[q].y)); }
  const float var = rbf(xsum32(m2) * (1.f / 640.f));
  const float rs = __builtin_amdgcn_rcpf(rbf(__builtin_amdgcn_sqrtf(rbf(var + 1e-5f))));
  *(LAS v2u*)(c.L + L_ST + w * 256 + t32 * 8) = (v2u){pk2(-mean, -mean), f2u(rs)};
}
DI void colsum(const Ctx& c, int t, int lane) {
  const int i = (c.w & 1) * 64 + lane;
  unsigned cs[20];
#pragma unroll
  for (int k = 0; k < 20; ++k) cs[k] = __builtin_amdgcn_raw_buffer_load_b32(c.rs, i * 4 + (k & 7) * 512, OFF_CS + (t * 16 + c.pj) * 10240 + (k >> 3) * 4096, SC1);
  float s = 0.f;
#pragma unroll
  for (int k = 0; k < 20; ++k) s += u2f(cs[k]);
  const float hi = rbf(s), mid = rbf(s - hi), lo = rbf(s - hi - mid);
  *(LAS v2u*)(c.L + L_CS + t * 1024 + i * 8) = (v2u){pk2(hi, mid), pk2(lo, 0.f)};
}
template <bool ISB>
DI void layer_c(const Ctx& c, const Params& p, const int lyr) {
  unsigned xp[10]; int xrv, xr4; v4u xc[3];
  int lane = lane_id();
  const int w = c.w, t32 = lane & 31, hh = lane >> 5, t16 = lane & 15, g = lane >> 4;
  constexpr int NQ = ISB ? 10 : 5, NKS = ISB ? 5 : 3, TRS = ISB ? 160 : 96;
  constexpr int type = ISB ? 1 : 0;
  LAS char* L = c.L;
  int l16 = lane * 16;
  LAS char* wa = L + l16;
  const int lb = __builtin_amdgcn_readfirstlane(lyr * LSZ);
  v4u xf[40];
  const int xs = lb + R_X + (c.pi * 4 + w) * 40960;
  const int q4 = (lane & 15) >> 2, p4 = lane & 3;
  LAS char* wh = wa + 51200;
  constexpr int VO = ISB ? 80 : 48;
  LAS char* dst = L + hh * 20480 + (32 * w + t32) * TRS;
  LAS char* ta = L + (w & 1) * 20480 + (4 * g + q4) * TRS + 8 * p4;
  LAS char* tb = ta + VO + 32 * (w >> 1) - (((w >> 1) && p4 >= 2) ? 16 : 0);
  int kvv = 16 * (w >> 1) + t16 < 21 ? g * 336 + (16 * (w >> 1) + t16) * 16 : OOR;
  int kvw = g < NQ % 4 ? kvv : OOR;
  LAS char* csa = L + (g ? L_ZERO : L_CS + type * 1024 + t16 * 64);
  LAS char* wq = L + L_ST + w * 256 + t16 * 8;
  LAS char* rsa = L + L_ST + w * 256 + t32 * 8 + 4;
  OPQV(wh); OPQV(dst); OPQV(ta); OPQV(tb); OPQV(kvv); OPQV(kvw); OPQV(csa); OPQV(wq); OPQV(rsa);
  SB();
  if (ISB || lyr) { VMWAIT(1); __builtin_amdgcn_s_sleep(PD1); }
  constexpr int XP = XPN, XR = ISB ? XRN : XRA;
#pragma unroll
  for (int k = 0; k < XP; ++k) { xf[k] = ldi(c.rs, l16, xs, k * 1024); SB(); }
  if (!ISB) VMWAIT(XP);
  RAWBAR();
  f32x4 a[5][2] = {};
  constexpr int PA = PAN;
  bf16x8 A[PA];
#pragma unroll
  for (int t = 0; t < 5; ++t) { OPQV(a[t][0]); OPQV(a[t][1]); }
  SB();
#pragma unroll
  for (int n = 0; n < PA; ++n) A[n] = rd(wa, n * 1024);
  SB();
  float rs = 0.f; v4u cs[4], mlo[2], mhi[2]; unsigned mm[2];
  LAS char* wq2;
  unsigned mk = chk128<4>(xf);
  sfor<0, 100>([&](auto N) AI {
    constexpr int n = decltype(N)::v, s = n / 5, t = n % 5, gq = n / 10, u = n % 10;
    if constexpr (u == 0) { fixup<4, gq, (XP / 4 + gq < 10 ? XP / 4 + gq : 10)>(c, xf, mk, l16, xs, 1024); SB(); }
    if constexpr ((n & 1) == 0) { OPQV(A[(n + 1) % PA]); SB(); }
    MF16(a[t][0], A[n % PA], asb(xf[2 * s])); SB();
    if constexpr ((u == 0 || u == 2 || u == 4 || u == 8) && XP + 4 * gq + (u == 8 ? 3 : u / 2) < 40) { constexpr int k = XP + 4 * gq + (u == 8 ? 3 : u / 2); xf[k] = ldi(c.rs, l16, xs, k * 1024); }
    if constexpr (gq < 9 && (u == 5 || u == 7)) { mk = mx4(u == 5 ? 0u : mk, xf[4 * gq + u]); OPQV(mk); }
    if constexpr (n == 70) { mm[0] = *(LAS unsigned*)(wq); mm[1] = *(LAS unsigned*)(wq + 128); }
    if constexpr (n == 71) { rs = *(LAS float*)(rsa); }
    if constexpr (n == 72) { cs[0] = *(LAS v4u*)(csa); }
    if constexpr (n == 73) { cs[1] = *(LAS v4u*)(csa + 16); }
    if constexpr (n == 74) { cs[2] = *(LAS v4u*)(csa + 32); }
    if constexpr (n == 84 || n == 85) { mlo[n - 84].z = vzero(); mlo[n - 84].w = vzero(); }
    if constexpr (n == 86 || n == 87) { mhi[n - 86].x = vzero(); mhi[n - 86].y = vzero(); }
    if constexpr (n == 88) { OPQV(mlo[0]); OPQV(mlo[1]); OPQV(mhi[0]); OPQV(mhi[1]); }
    if constexpr (n == 80 || n == 81) { mlo[n - 80].x = mm[n - 80]; mlo[n - 80].y = mm[n - 80]; }
    if constexpr (n == 82 || n == 83) { mhi[n - 82].z = mm[n - 82]; mhi[n - 82].w = mm[n - 82]; }
    SB();
    MF16(a[t][1], A[n % PA], asb(xf[2 * s + 1])); SB();
    if constexpr (n + PA < 100) A[n % PA] = (n + PA < 50) ? rd(wa, (n + PA) * 1024) : rd(wh, (n + PA - 50) * 1024);
    if constexpr (gq < 9 && (u == 6 || u == 8)) { mk = mx4(mk, xf[4 * gq + u - 2]); OPQV(mk); }
    SB();
    if constexpr (n >= 90 && n < 95) {
      constexpr int q = (n + 2) % 5;
      MF16(a[q][0], asb(cs[q >> 1]), asb((q & 1) ? mhi[0] : mlo[0])); SB();
      MF16(a[q][1], asb(cs[q >> 1]), asb((q & 1) ? mhi[1] : mlo[1])); SB();
    }
    if constexpr (n == B4N) RAWBAR();
  });
  MFIN();
#pragma unroll
  for (int t = 0; t < 5; ++t) { OPQV(a[t][0]); OPQV(a[t][1]); }
  SB();
  {
    float kx[20], vx[20];
#pragma unroll
    for (int i = 0; i < 20; ++i) swap16(a[i >> 2][0][i & 3], a[i >> 2][1][i & 3], kx[i], vx[i]);
    unsigned v[10], o[ISB ? 20 : 10];
#pragma unroll
    for (int i = 0; i < 10; ++i) v[i] = pk2(vx[2 * i] * rs, vx[2 * i + 1] * rs);
    *(LAS v4u*)(dst + VO) = (v4u){v[0], v[1], v[2], v[3]}; *(LAS v4u*)(dst + VO + 16) = (v4u){v[4], v[5], v[6], v[7]};
    *(LAS v4u*)(dst + VO + 32) = (v4u){v[8], v[9], vone(), vzero()};
    phi20<ISB>(kx, rs, o);
#pragma unroll
    for (int i = 0; i < (ISB ? 5 : 2); ++i) *(LAS v4u*)(dst + 16 * i) = (v4u){o[4 * i], o[4 * i + 1], o[4 * i + 2], o[4 * i + 3]};
    if (!ISB) *(LAS v2u*)(dst + 32) = (v2u){o[8], o[9]};
  }
  SB();
  bar();
  {
    constexpr int NF = ISB ? 3 : 2;
    bf16x8 fa[4][NF], fb[4];
    f32x4 kv[NF] = {};
#pragma unroll
    for (int ks = 0; ks < 6; ++ks) {
      if (ks < 4) {
        fb[ks] = tr2(tb, 32 * ks * TRS, (32 * ks + 16) * TRS);
#pragma unroll
        for (int ft = 0; ft < NF; ++ft) fa[ks][ft] = tr2(ta, 32 * ks * TRS + 32 * ft, (32 * ks + 16) * TRS + 32 * ft);
        SB();
      }
      if (ks >= 1 && ks - 1 < XE && ks - 1 < XR) { xf[ks - 1] = ldi(c.rs, l16, xs, (ks - 1) * 1024); SB(); }
      if (ks >= 2) {
#pragma unroll
        for (int ft = NF - 1; ft >= 0; --ft) { kv[ft] = mfma16(fa[ks - 2][ft], fb[ks - 2], kv[ft]); OPQV(kv[ft]); }
        SB();
      }
    }
    const int kvs = lb + c.ko + 512 + (w & 1) * NQ * 336;
    wq = wa + L_Q; wq2 = L + L_Q + 2048 + g * 128 + (t16 >> 3) * 64 + (t16 & 3) * 16;
    xrv = x_voff(c, lane); xr4 = (g & 1) ? OOR : xrv;
    OPQV(wq); OPQV(wq2); OPQV(xrv); OPQV(xr4);
    SB();
#pragma unroll
    for (int ft = NF - 1; ft >= 0; --ft) st128(c.rs, (ft < NF - 1 ? kvv : kvw) + ft * 1344, kvs, __builtin_bit_cast(v4u, kv[ft]));
  }
  RAWBAR();
  int k0, k1, ov, xov;
  unsigned pq[NKS * 4];
  {
    f32x4 q[3][2] = {};
#pragma unroll
    for (int t = 0; t < 3; ++t) { OPQV(q[t][0]); OPQV(q[t][1]); }
    SB();
    auto rdq = [&](int n) AI { const int s = (n / 3 + XR / 2) % 20, t = n % 3; return t < 2 ? rd(wq, s * 2560 + t * 1024) : rd(wq2, s * 2560); };
#pragma unroll
    for (int n = 0; n < PA; ++n) A[n] = rdq(n);
    cs[2] = *(LAS v4u*)(csa + 32); cs[3] = *(LAS v4u*)(csa + 48);
    OPQV(mm[0]); OPQV(mm[1]);
#pragma unroll
    for (int h = 0; h < 2; ++h) { mlo[h] = (v4u){mm[h], mm[h], vzero(), vzero()}; mhi[h] = (v4u){vzero(), vzero(), mm[h], mm[h]}; OPQV(mlo[h]); OPQV(mhi[h]); }
    SB();
    sfor<0, 60>([&](auto N) AI {
      constexpr int n = decltype(N)::v, s = (n / 3 + XR / 2) % 20, t = n % 3;
      if constexpr ((n & 1) == 0) { OPQV(A[(n + 1) % PA]); SB(); }
      MF16(q[t][0], A[n % PA], asb(xf[2 * s])); SB();
      if constexpr (n % RQ == 0 && XE + n / RQ < XR) xf[XE + n / RQ] = ldi(c.rs, l16, xs, (XE + n / RQ) * 1024);
      if constexpr (n == 41 || n == 44 || n == 47) { constexpr int k = (n - 41) / 3; xc[k] = ld128(c.rs, k < 2 ? xrv : xr4, lb + x_piece(c, 2 * k)); }
      SB();
      MF16(q[t][1], A[n % PA], asb(xf[2 * s + 1])); SB();
      if constexpr (n + PA < 60) A[n % PA] = rdq(n + PA);
      SB();
      if constexpr (n >= 6 && n < 9) {
        constexpr int z = (n + 1) % 3;
        MF16(q[z][0], asb(cs[(5 + z) >> 1]), asb((z & 1) ? mlo[0] : mhi[0]));
        MF16(q[z][1], asb(cs[(5 + z) >> 1]), asb((z & 1) ? mlo[1] : mhi[1]));
        SB();
      }
    });
    MFIN();
#pragma unroll
    for (int t = 0; t < 3; ++t) { OPQV(q[t][0]); OPQV(q[t][1]); }
    SB();
    {
      const int e0 = 4 * (t32 >> 3) + (t32 & 3), e1 = min(16 + e0, 20);
      const bool own = ((t32 >> 2) & 1) == hh;
      k0 = own ? L_KV + hh * 2016 + e0 * 96 : L_ZERO; k1 = own ? L_KV + hh * 2016 + e1 * 96 : L_ZERO;
      xov = (t32 >> 4) * 20480 + t16 * 16; ov = xov + 768 * hh; xov = hh ? OOR : xov;
      OPQV(k0); OPQV(k1); OPQV(ov); OPQV(xov);
    }
    float qx[24];
#pragma unroll
    for (int i = 0; i < 12; ++i) swap16(q[i >> 2][0][i & 3], q[i >> 2][1][i & 3], qx[i], qx[12 + i]);
    phi20<ISB>(qx, rs, pq);
#pragma unroll
    for (int i = 0; i < (ISB ? 20 : 10); ++i) OPQV(pq[i]);
    if (!ISB) { pq[10] = vzero(); pq[11] = vzero(); }
  }
  f32x4 acc[5];
  {
    unsigned d0, d1;
#pragma unroll
    for (int k = 0; k < 2; ++k) { uswap16(xc[k].x, xc[k].z, xp[4 * k], xp[4 * k + 2]); uswap16(xc[k].y, xc[k].w, xp[4 * k + 1], xp[4 * k + 3]); }
    uswap16(xc[2].x, xc[2].z, xp[8], d0); uswap16(xc[2].y, xc[2].w, xp[9], d1);
  }
#pragma unroll
  for (int ct = 0; ct < 5; ++ct) { acc[ct] = (f32x4){blo(xp[2 * ct]), bhi(xp[2 * ct]), blo(xp[2 * ct + 1]), bhi(xp[2 * ct + 1])}; OPQV(acc[ct]); }
  RAWBAR();
  {
    bf16x8 f0[NKS], f1[NKS];
#pragma unroll
    for (int ks = 0; ks < NKS; ++ks) f1[ks] = rd(L + k1, 16 * ks);
#pragma unroll
    for (int ks = 0; ks < NKS; ++ks) f0[ks] = rd(L + k0, 16 * ks);
    SB();
    f32x16 n0 = {}, n1 = {};
#pragma unroll
    for (int ks = 0; ks < NKS; ++ks) MF32(n1, f1[ks], asb((v4u){pq[4 * ks], pq[4 * ks + 1], pq[4 * ks + 2], pq[4 * ks + 3]}));
    float id = 0.f;
    const int os = lb + c.oo;
#pragma unroll
    for (int ks = 0; ks < NKS; ++ks) {
      MF32(n0, f0[ks], asb((v4u){pq[4 * ks], pq[4 * ks + 1], pq[4 * ks + 2], pq[4 * ks + 3]}));
      if (ks == NKS - 2) { OPQV(n1); id = __builtin_amdgcn_rcpf(n1[4] + 1e-6f); OPQV(id); }
      if (ks == NKS - 1) {
        const unsigned m0 = pk2(n1[0] * id, n1[1] * id), m1 = pk2(n1[2] * id, n1[3] * id);
        auto r0 = __builtin_amdgcn_permlane32_swap(m0, m0, false, false); auto r1 = __builtin_amdgcn_permlane32_swap(m1, m1, false, false);
        st128(c.rs, xov + 512, os, (v4u){r0[0], r1[0], r0[1], r1[1]});
      }
      SB();
    }
    unsigned d[8];
#pragma unroll
    for (int i = 0; i < 8; ++i) d[i] = pk2(n0[2 * i] * id, n0[2 * i + 1] * id);
    st128(c.rs, ov, os, (v4u){d[0], d[1], d[2], d[3]});
    st128(c.rs, ov + 256, os, (v4u){d[4], d[5], d[6], d[7]});
  }
  RAWBAR();
  constexpr int WPD = 32;
  bf16x8 wf[WPD];
#pragma unroll
  for (int k = 0; k < WPD; ++k) wf[k] = rd(wa, k * 1024);
  SB();
  VMWAIT(0);
  __builtin_amdgcn_s_sleep(PD2);
  v4u of[20];
  const int oos = lb + R_O + (c.pi * 8 + 4 * c.th + w) * 20480;
#pragma unroll
  for (int s = 0; s < OPF; ++s) { of[s] = ldi(c.rs, l16, oos, s * 1024); SB(); }
  mk = chk128<5>(of);
  sfor<0, 100>([&](auto N) AI {
    constexpr int n = decltype(N)::v, s = n / 5, ct = n % 5, gq = n / 25, u = n % 25;
    if constexpr (u == 0) { fixup<5, gq, ((OPF + 6 * gq) / 5 < 4 ? (OPF + 6 * gq) / 5 : 4)>(c, of, mk, l16, oos, 1024); SB(); }
    if constexpr (n >= WPD && (n & 3) == 0) { OPQV(wf[(n + 3) % WPD]); SB(); }
    MF16(acc[ct], wf[n % WPD], asb(of[s])); SB();
    if constexpr (n % 4 != 3) { constexpr int m = WPD + 3 * (n / 4) + n % 4; if constexpr (m < 100) wf[m % WPD] = (m < 50) ? rd(wa, m * 1024) : rd(wh, (m - 50) * 1024); }
    if constexpr (n % 4 == 3 && OPF + n / 4 < 20) of[OPF + n / 4] = ldi(c.rs, l16, oos, (OPF + n / 4) * 1024);
    if constexpr (gq < 3 && u >= 14 && u < 24 && (u & 1) == 0) { constexpr int q = (u - 14) / 2; mk = mx4(q == 0 ? 0u : mk, of[5 * gq + 5 + q]); OPQV(mk); }
    SB();
  });
  MFIN();
#pragma unroll
  for (int t = 0; t < 5; ++t) OPQV(acc[t]);
  SB();
#pragma unroll
  for (int ct = 0; ct < 5; ++ct) { xp[2 * ct] = pk2(acc[ct][0], acc[ct][1]); xp[2 * ct + 1] = pk2(acc[ct][2], acc[ct][3]); }
  x_chunks(xp, xc);
  if (lyr < 3) {
    publish_x(c, lb + LSZ, xrv, xr4, xc);
    RAWBAR();
    publish_stats(c, lb + LSZ, lane, xp);
  } else {
    const rsrc_t ro = __builtin_amdgcn_make_buffer_rsrc((void*)p.out, 0, 2048 * 1280, 0x00020000);
    const int ov2 = t16 * 1280 + 32 * (g & 1) + 16 * (g >> 1), os2 = (128 * c.pi + 64 * c.th + 16 * w) * 1280 + 160 * c.cs;
#pragma unroll
    for (int k = 0; k < 3; ++k) { __builtin_amdgcn_raw_buffer_store_b128(xc[k], ro, (k == 2 && (g & 1)) ? OOR : ov2 + 64 * k, os2, 0); asm volatile("s_nop 1" :: "v"(xc[k])); }
  }
  SB();
}

template <bool ISB>
DI void layer_h(const Ctx& c, const int lyr) {
  int lane = lane_id();
  const int w = c.w, t32 = lane & 31, hh = lane >> 5;
  constexpr int NQ = ISB ? 10 : 5, NP = 2 * NQ * 21;
  constexpr int type = ISB ? 1 : 0;
  LAS char* L = c.L;
  int l16 = lane * 16;
  LAS char* wa = L + l16;
  LAS char* sd = wa + w * 25600;
  const int lb = __builtin_amdgcn_readfirstlane(lyr * LSZ);
  int wos = OFF_WO + (type * 8 + c.cs) * WO_SZ + w * 25600;
  int wt0 = OFF_WT + ((1 - type) * 16 + c.pj) * WT_SZ;
  if (lyr == 3) wt0 = OOR - 0x1000000;
  OPQ(wos); OPQ(wt0);
  if (ISB || lyr) bar();
  if (ISB || lyr) row_stats(c, lb, lane);
  if (!ISB && lyr == 0) {
    v4u u0[13];
#pragma unroll
    for (int k = 0; k < 13; ++k) u0[k] = ldi(c.rs, l16, OFF_WT + c.pj * WT_SZ + w * 12800, 102400 + k * 1024);
    VMWAIT(13);
    int f0 = F_H + ((c.pj * 4 + w) * 16 + c.pi) * 16, f1 = F_R + ((c.pi * 4 + w) * 16 + c.pj) * 16; OPQ(f0); OPQ(f1);
    set_flag(c, lane == 0 ? f0 : (lane == 1 ? f1 : OOR));
    if (w == 0 || w == 3) colsum(c, 0, lane);
    LAS char* ud = wa + 102400 + w * 12800;
#pragma unroll
    for (int k = 0; k < 13; ++k) stw(k < 12 ? ud + k * 1024 : (hh ? L + L_DUMP : ud + 12 * 1024), u0[k]);
  }
  constexpr int NR = ISB ? 2 : 1, PW = NP / 4 + (ISB ? 0 : 1);
  int gv[NR], kvl[NR];
#pragma unroll
  for (int r = 0; r < NR; ++r) {
    const int pn = lane + 64 * r, pc = w * PW + pn; const bool ok = pn < PW && pc < NP;
    const int hf = (pc * 3121) >> 16, zi = w * 11 + lane - 53;
    gv[r] = ok ? pc * 16 : OOR;
    kvl[r] = ok ? L_KV + (pc - hf * 21) * 96 + hf * 8 + (hf >= NQ ? 2016 - NQ * 8 : 0) : ((!ISB && lane >= 53 && zi < 42) ? L_KV + zi * 96 + 40 : L_DUMP);
    OPQV(gv[r]); OPQV(kvl[r]);
  }
  v4u wst[25], up[13], nxt[25];
  auto bg = [&](int n) AI {
    if (n < 25) wst[n] = ldi(c.rs, l16, wos, n * 1024);
    else if (n < 38) up[n - 25] = ldi(c.rs, l16, wt0 + w * 12800, 102400 + (n - 25) * 1024);
    else if (n < 63) nxt[n - 38] = ldi(c.rs, l16, wt0 + w * 25600, (n - 38) * 1024);
    SB();
  };
  if (ISB || lyr) {
#pragma unroll
    for (int n = 0; n < Q1; ++n) { bg(n); __builtin_amdgcn_s_sleep(SL1); }
  }
  bar();
  const unsigned t4 = (unsigned)__builtin_amdgcn_s_memrealtime();
  const int hb = OFF_HB + lyr * 8192;
  if (w == 0) __builtin_amdgcn_raw_buffer_store_b64((v2u){t4, c.ep.x}, c.rs, lane == 0 ? 0 : OOR, hb + (c.pi * 16 + c.pj) * 8, SC1);
  if (!ISB && lyr == 0) {
    wait_flags(c, (lane & 15) * 32 + (lane >> 5) * 8192 + 16, F_A + (c.pj & 7) * 1024);
#pragma unroll
    for (int n = 0; n < Q1; ++n) bg(n);
  }
  if (lyr < 2) poison_all(c, lb + 2 * LSZ, ISB ? 7 : 6, l16, lane);
#pragma unroll
  for (int n = Q1; n < Q2; ++n) bg(n);
  v4u hv[2];
  const int hvo = w == 3 ? l16 : OOR;
  RAWBAR();
#pragma unroll
  for (int q = 0; q < 2; ++q) hv[q] = ld128(c.rs, hvo, hb + q * 1024);
  RAWBAR();
  const unsigned t6 = (unsigned)__builtin_amdgcn_s_memrealtime();
  v4u kg[NR][16];
  const int gs = lb + R_KV + c.pj * 16 * KVSLOT + 512;
  if (!ISB && lyr == 0) wait_flags(c, l16, F_H + c.pj * 1024);
  {
    volatile LAS unsigned* go = (volatile LAS unsigned*)(L + L_GO) + (lyr & 1);
    int lag = 0;
    if (w == 3) {
      NOUNROLL for (unsigned spins = 0; spins < 64; ++spins) {
        bool ok = true; lag = 0;
#pragma unroll
        for (int q = 0; q < 2; ++q) { ok &= hv[q].y == c.ep.x && hv[q].w == c.ep.x; lag = max(max(lag, (int)(hv[q].x - t4)), (int)(hv[q].z - t4)); }
        if (__all(ok)) break;
#pragma unroll
        for (int q = 0; q < 2; ++q) hv[q] = ld128(c.rs, hvo, hb + q * 1024);
        lag = 0;
      }
#pragma unroll
      for (int d = 32; d >= 1; d >>= 1) lag = max(lag, __shfl_xor(lag, d));
      lag = min(lag, 400);
      *go = ((unsigned)(lyr + 1) << 16) | (unsigned)lag;
    }
#pragma unroll
  for (int k = 0; k < WE; ++k) { if (Q2 + k < Q4) bg(Q2 + k); stw(sd + k * 1024, wst[k]); SB(); }
    if (w != 3) {
      unsigned v = 0;
      NOUNROLL for (unsigned spins = 0; spins < 4096; ++spins) { v = *go; if ((v >> 16) == (unsigned)(lyr + 1)) break; __builtin_amdgcn_s_sleep(1); }
      lag = v & 0x1FF;
    }
    lag = __builtin_amdgcn_readfirstlane(lag) + KM;
    NOUNROLL while ((int)((unsigned)__builtin_amdgcn_s_memrealtime() - t6) < lag) __builtin_amdgcn_s_sleep(1);
  }
#pragma unroll
  for (int n = 0; n < 16 * NR; ++n) { kg[n % NR][n / NR] = ld128(c.rs, gv[n % NR], gs + (n / NR) * KVSLOT); SB(); }
  {
    f32x4 sum[NR];
    bool redo = false;
#pragma unroll
    for (int i = 0; i < 16; ++i) {
      unsigned m = 0;
#pragma unroll
      for (int r = 0; r < NR; ++r) { m = mx4(m, kg[r][i]); sum[r] = i ? sum[r] + __builtin_bit_cast(f32x4, kg[r][i]) : __builtin_bit_cast(f32x4, kg[r][i]); OPQV(sum[r]); }
      if (__builtin_expect(__any(m == POISON), 0)) {
        redo = true;
        int gs2 = gs; OPQ(gs2);
#pragma unroll
        for (int r = 0; r < NR; ++r) kg[r][i] = ld128(c.rs, gv[r], gs2 + i * KVSLOT);
      }
    }
    if (__builtin_expect(redo, 0)) {
      NOUNROLL for (unsigned spins = 0; spins < SPIN_CAP; ++spins) {
        bool any = false;
        int gs2 = gs; OPQ(gs2);
        __builtin_amdgcn_s_waitcnt(0x0F70);
#pragma unroll
        for (int i = 0; i < 16; ++i)
#pragma unroll
          for (int r = 0; r < NR; ++r) sum[r] = i ? sum[r] + __builtin_bit_cast(f32x4, kg[r][i]) : __builtin_bit_cast(f32x4, kg[r][i]);
        float t = sum[0][1] + sum[0][3];
        if (NR == 2) t += sum[NR - 1][1] + sum[NR - 1][3];
        if (!__any(t != t)) break;
#pragma unroll
        for (int i = 0; i < 16; ++i) {
          unsigned m = 0;
#pragma unroll
          for (int r = 0; r < NR; ++r) m = mx4(m, kg[r][i]);
          if (__any(m == POISON)) {
            any = true;
#pragma unroll
            for (int r = 0; r < NR; ++r) kg[r][i] = ld128(c.rs, gv[r], gs2 + i * KVSLOT);
          }
        }
        if (!any) break;
      }
      __builtin_amdgcn_s_waitcnt(0x0F70);
    }
#pragma unroll
    for (int r = 0; r < NR; ++r) *(LAS v2u*)(L + kvl[r]) = (v2u){pk2(sum[r][0], sum[r][1]), pk2(sum[r][2], sum[r][3])};
  }
  SB();
  bar();
#pragma unroll
  for (int k = WE; k < 25; ++k) stw(sd + k * 1024, wst[k]);
  SB();
  if (!ISB && lyr == 0 && w == 0) wait_flags(c, l16, F_R + c.pi * 1024);
  bar();
  if (lyr == 3) return;
#pragma unroll
  for (int n = Q4; n < Q5; ++n) bg(n);
  {
    LAS char* ud = wa + 102400 + w * 12800;
    LAS char* ul = hh ? L + L_DUMP : ud + 12 * 1024;
#pragma unroll
    for (int k = 0; k < 13; ++k) stw(k < 12 ? ud + k * 1024 : ul, up[k]);
  }
  SB();
  __builtin_amdgcn_s_sleep(HS);
  if (!ISB && lyr == 0 && w < 2) colsum(c, 1, lane);
#pragma unroll
  for (int n = Q5; n < 63; ++n) bg(n);
  RAWBAR();
#pragma unroll
  for (int k = 0; k < 25; ++k) stw(sd + k * 1024, nxt[k]);
  SB();
}

DI void set_ctx(Ctx& c, const Params& p, int b, char* smem) {
  c.L = (LAS char*)smem;
  c.rs = __builtin_amdgcn_make_buffer_rsrc(p.ws, 0, WS_USED, 0x00020000);
  const int xcd = b & 7, r = b >> 3;
  c.pi = 4 * (xcd >> 1) + (r & 3); c.pj = 8 * (xcd & 1) + (r >> 2); c.th = c.pj >> 3; c.cs = c.pj & 7;
  c.lane = threadIdx.x & 63; c.w = __builtin_amdgcn_readfirstlane(threadIdx.x >> 6) & 3;
  c.xo = R_X + (4 * c.pi + 2 * c.th + (c.w >> 1)) * 40960 + (c.w & 1) * 1024;
  c.oo = R_O + (c.pi * 8 + 2 * c.w) * 20480 + 5 * c.pj * 256;
  c.ko = R_KV + (c.pj * 16 + c.pi) * KVSLOT;
  OPQ(c.xo); OPQ(c.oo); OPQ(c.ko);
}
constexpr int CODE_KB = 48;
__global__ void __launch_bounds__(512, 2) mega(Params p) {
  extern __shared__ __attribute__((aligned(16))) char smem[];
  unsigned pf;
  {
    unsigned long long pc; asm volatile("s_getpc_b64 %0" : "=s"(pc));
    const unsigned ln = min(((blockIdx.x >> 3) & 3u) * 128u + (threadIdx.x >> 2), (unsigned)(CODE_KB * 8 - 1));
    asm volatile("global_load_dword %0, %1, %2" : "=v"(pf) : "v"(ln * 128u), "s"(pc & ~127ull) : "memory");
  }
  Ctx c;
  set_ctx(c, p, blockIdx.x, smem);
  const int w8 = __builtin_amdgcn_readfirstlane(threadIdx.x >> 6);
  LAS char* L = c.L;
  const int lane = c.lane, w = c.w, t16 = lane & 15, g = lane >> 4, q4 = t16 >> 2, p4 = lane & 3;
  int l16 = lane * 16;
  v2u ep;
  if (w8 < 4) {
    const rsrc_t rx = __builtin_amdgcn_make_buffer_rsrc((void*)p.x, 0, 2048 * 1280, 0x00020000);
    const int xa = t16 * 1280 + 32 * (g & 1) + 16 * (g >> 1);
    v4u xc[3];
#pragma unroll
    for (int k = 0; k < 3; ++k) xc[k] = __builtin_amdgcn_raw_buffer_load_b128(rx, (k == 2 && (g & 1)) ? OOR : xa + 64 * k, (128 * c.pi + 64 * c.th + 16 * w) * 1280 + 160 * c.cs, 0);
    ep = ld64(c.rs, 0, OFF_CTRL);
    const int xv = x_voff(c, lane);
    RAWBAR();
    publish_x(c, 0, xv, (g & 1) ? OOR : xv, xc);
    unsigned xp[10], d0, d1;
#pragma unroll
    for (int k = 0; k < 2; ++k) { xp[4 * k] = xc[k].x; xp[4 * k + 1] = xc[k].y; xp[4 * k + 2] = xc[k].z; xp[4 * k + 3] = xc[k].w; }
    uswap16(xc[2].x, xc[2].z, xp[8], d0); uswap16(xc[2].y, xc[2].w, xp[9], d1);
    publish_stats(c, 0, lane, xp);
    c.ep = next_epoch(ep);
    int f0 = F_X + (((c.pi * 2 + c.th) * 4 + w) * 8 + c.cs) * 16; OPQ(f0);
    DRAIN();
    set_flag(c, lane == 0 ? f0 : OOR);
    __builtin_amdgcn_s_sleep(8);
    wait_flags(c, t16 * 16, F_X + (c.pi * 4 + w) * 256);
    row_stats(c, 0, lane);
    int bq = c.oo + 1024, dq = c.oo + 1024 + 20480 - 256; OPQ(bq); OPQ(dq);
    poison128(c.rs, l16, c.oo); poison128(c.rs, l16, c.oo + 20480);
    poison128(c.rs, lane < 32 ? l16 + (lane < 16 ? bq : dq) : OOR, 0);
    poison128(c.rs, l16, c.ko + 512 + w * 1024);
    SB();
    poison_all(c, LSZ, 7, l16, lane);
  } else {
    LAS char* S = L + L_Q + w * 12800;
    const int kind = w == 2 ? 2 : (w == 3 ? (c.pi < 8 ? c.pi >> 2 : 3) : w), kb = w == 3 ? 16 + (c.pi & 3) : c.pi;
    const rsrc_t rw = __builtin_amdgcn_make_buffer_rsrc((void*)(kind == 2 ? (c.th ? p.wo[1] : p.wo[0]) : (kind ? p.wqkv[1] : p.wqkv[0])), 0, kind == 3 ? 0 : (kind == 2 ? 640 * 1280 : 640 * 3840), 0x00020000);
    const int vq = kind == 2 ? (t16 < 10 ? g * 1280 + t16 * 16 : OOR) : (t16 < 15 ? g * 3840 + t16 * 16 + (t16 >= 5 ? 1200 : 0) + (t16 >= 10 ? 1200 : 0) : OOR);
    const int so = kind == 2 ? c.cs * 160 + 40 * kb * 1280 : c.pj * 80 + 32 * kb * 3840, st = kind == 2 ? 4 * 1280 : 4 * 3840;
    if (kind) __builtin_amdgcn_s_sleep(PW0);
    v2u cp[8];
    {
      v4u cw[10];
#pragma unroll
      for (int k = 0; k < 10; ++k) cw[k] = __builtin_amdgcn_raw_buffer_load_b128(rw, vq, (k < 8 || kind == 2) ? so + k * st : OOR, 0);
      ep = ld64(c.rs, 0, OFF_CTRL);
      if (kind == 1) {
        const rsrc_t rp = __builtin_amdgcn_make_buffer_rsrc((void*)p.wphi, 0, 32 * 800, 0x00020000);
        const int d = lane >> 3, q8 = lane & 7;
#pragma unroll
        for (int k = 0; k < 8; ++k) cp[k] = __builtin_amdgcn_raw_buffer_load_b64(rp, (d + 8 * (k & 3) < 20 && q8 < 5) ? d * 40 + q8 * 8 : OOR, (2 * c.pj + (k >> 2)) * 800 + (k & 3) * 320, 0);
      }
      if (w == 0 && lane < 4) *(LAS unsigned*)(L + L_GO + 4 * lane) = 0u;
      SB();
      bar();
      SB();
#pragma unroll
      for (int k = 0; k < 10; ++k) *(LAS v4u*)(S + l16 + k * 1024) = cw[k];
    }
    c.ep = next_epoch(ep);
    if (w == 0 && lane < 24) *(LAS unsigned*)(L + L_ZERO + 4 * lane) = 0u;
    if (kind == 1) {
#pragma unroll
        for (int k = 0; k < 8; ++k) *(LAS v2u*)(S + 8192 + lane * 8 + k * 512) = cp[k];
        LAS char* pa = S + 8192 + 64 * (8 * g + q4) + 8 * p4;
        LAS char* pr = S + t16 * 256;
        LAS char* pb = pr + (g < 3 ? g * 16 : 0); LAS char* pb2 = pb + (g == 2 ? 0 : 8);
        f32x4 o0[8], o1[8];
#pragma unroll
        for (int u = 0; u < 8; ++u) {
          const bf16x8 f0 = tr2(pa + (u & 1) * 2048, 0, 256), f1 = tr2(pa + (u & 1) * 2048, 32, 288);
          const v2u lo = *(LAS v2u*)(pb + (u & 3) * 40 + (u >> 2) * 4096), hi = *(LAS v2u*)(pb2 + (u & 3) * 40 + (u >> 2) * 4096);
          const bf16x8 bb = asb((v4u){lo.x, lo.y, hi.x, hi.y});
          o0[u] = mfma16(f0, bb, (f32x4){}); o1[u] = mfma16(f1, bb, (f32x4){});
        }
        SB();
#pragma unroll
        for (int u = 0; u < 8; ++u) {
          *(LAS v2u*)(pr + 8 * g + (u & 3) * 40 + (u >> 2) * 4096) = (v2u){pk2(o0[u][0], o0[u][1]), pk2(o0[u][2], o0[u][3])};
          *(LAS v2u*)(g == 0 ? pr + 32 + (u & 3) * 40 + (u >> 2) * 4096 : S + 12288) = (v2u){pk2(o1[u][0], o1[u][1]), pk2(o1[u][2], o1[u][3])};
        }
        SB();
    }
    v4u f[8];
    if (kind != 2) {
      LAS char* pt = S + (8 * g + q4) * 256 + (p4 >> 1) * 40;
      const int hd = p4 >> 1;
#pragma unroll
      for (int T = 0; T < 8; ++T) {
        const int sq = T < 5 ? T : ((p4 & 1) ? (T < 7 ? T - 2 : 2) : T - 5);
        const int qd = sq + hd >= 5 ? sq + hd - 5 : sq + hd;
        f[T] = __builtin_bit_cast(v4u, tr2(pt + (T < 5 ? 80 + 80 * (p4 & 1) : 0) + 8 * qd, 0, 1024));
      }
    } else {
#pragma unroll
      for (int k = 0; k < 8; ++k) {
        const int gc = min(4 * k + g, 24), rg = (gc * 13) >> 6, ct = gc - rg * 5;
        f[k] = __builtin_bit_cast(v4u, tr2(S + (8 * rg + q4) * 256 + 32 * ct + 8 * p4, 0, 1024));
      }
    }
    SB();
    if (kind == 2) {
#pragma unroll
      for (int k = 0; k < 7; ++k) {
        const int gi = 4 * k + g, rg = (gi * 13) >> 6, ct = gi - rg * 5, RG = 5 * c.pi + rg;
        st128r(c.rs, gi < 25 ? (((RG >> 2) * 5 + ct) * 64 + (RG & 3) * 16 + t16) * 16 : OOR, OFF_WO + c.pj * WO_SZ, f[k]);
      }
      DRAIN();
      int f0 = F_A + (c.pj * 32 + c.pi) * 32 + 16; OPQ(f0);
      set_flag(c, lane == 0 ? f0 : OOR);
    }
    v4u im[25];
    volatile LAS unsigned* go = (volatile LAS unsigned*)(L + L_GO + 8);
    NOUNROLL for (int pass = 0; pass < 2; ++pass) {
      if (pass == 1) {
        if (w == 2) {
          wait_flags(c, min(lane, 19) * 32, F_A + c.pj * 1024);
          go[0] = 1u;
        } else {
          NOUNROLL for (unsigned spins = 0; spins < (SPIN_CAP << 4); ++spins) { if (go[0]) break; __builtin_amdgcn_s_sleep(1); }
          asm volatile("" ::: "memory");
        }
#pragma unroll
        for (int k = 0; k < 25; ++k) im[k] = ldi(c.rs, l16, OFF_WT + c.pj * WT_SZ + w * 25600, k * 1024);
        SB();
#pragma unroll
        for (int k = 0; k < 25; ++k) *(LAS v4u*)(L + w * 25600 + l16 + k * 1024) = im[k];
        bar();
      }
      if (pass == kind) {
        const int tv7 = (t16 & 4) ? OOR : g * 128 + (t16 >> 3) * 64 + (t16 & 3) * 16, ib = OFF_WT + (16 * kind + c.pj) * WT_SZ;
#pragma unroll
        for (int T = 0; T < 8; ++T) st128r(c.rs, T < 7 ? l16 : tv7, ib + (T < 5 ? (kb * 5 + T) * 1024 : L_Q + kb * 2560 + (T - 5) * 1024), f[T]);
        float sm[8];
#pragma unroll
        for (int T = 0; T < 8; ++T) sm[T] = xsum32(xsum16(sum2(f[T].x, sum2(f[T].y, sum2(f[T].z, sum2(f[T].w, 0.f))))));
        st128r(c.rs, g < 2 ? t16 * 32 + g * 16 : OOR, OFF_CS + (kind * 16 + c.pj) * 10240 + kb * 512, g ? (v4u){f2u(sm[4]), f2u(sm[5]), f2u(sm[6]), f2u(sm[7])} : (v4u){f2u(sm[0]), f2u(sm[1]), f2u(sm[2]), f2u(sm[3])});
        if (kind == 0) {
          DRAIN();
          int f0 = F_A + (c.pj * 32 + kb) * 32; OPQ(f0);
          set_flag(c, lane == 0 ? f0 : OOR);
        }
      }
    }
  }
  asm volatile("" :: "v"(pf));
  int role = w8; OPQ(role);
  if (role < 4) {
    for (int it = 0; it < 2; ++it) {
      layer_c<false>(c, p, 2 * it);
      layer_c<true>(c, p, 2 * it + 1);
    }
    if (blockIdx.x == 0 && w == 0) set_flag(c, lane_id() == 0 ? 0 : OOR);
  } else {
    for (int it = 0; it < 2; ++it) {
      layer_h<false>(c, 2 * it);
      layer_h<true>(c, 2 * it + 1);
    }
  }
}

extern "C" void kernel_launch(void* const* d_in, const int* in_sizes, int n_in, void* d_out, int out_size, void* d_ws, size_t ws_size, hipStream_t stream) {
  Params p;
  p.x = (const unsigned short*)d_in[0];
  p.wqkv[0] = (const unsigned short*)d_in[1]; p.wo[0] = (const unsigned short*)d_in[2];
  p.wqkv[1] = (const unsigned short*)d_in[4]; p.wphi = (const unsigned short*)d_in[5]; p.wo[1] = (const unsigned short*)d_in[6];
  p.ws = (unsigned char*)d_ws; p.out = (unsigned short*)d_out;
  (void)hipFuncSetAttribute((const void*)mega, hipFuncAttributeMaxDynamicSharedMemorySize, LDS_BYTES);
  mega<<<256, 512, LDS_BYTES, stream>>>(p);
}
```
